```python
import torch

def _softmax(x, axis=-1):
    return torch.softmax(x, dim=axis)
def _gelu(x):
    return torch.nn.functional.gelu(x, approximate="tanh")
def _silu(x):
    return torch.nn.functional.silu(x)
def _sigmoid(x):
    return torch.sigmoid(x)
def _layernorm(x, axis=-1, eps=1e-5):
    m = x.mean(dim=axis, keepdim=True)
    v = x.var(dim=axis, keepdim=True, unbiased=False)
    return (x - m) / torch.sqrt(v + eps)
def _scatter_add(tgt, idx, src, axis=0):
    return tgt.clone().index_add_(axis, idx, src)
def _segment_sum(src, seg, n):
    out = torch.zeros((n,) + src.shape[1:], dtype=src.dtype)
    return out.index_add_(0, seg, src)
def _pad(x, pw):
    flat = tuple(v for pair in reversed(pw) for v in pair)
    return torch.nn.functional.pad(x, flat)

kernel_name = 'recurrent_scan__compressed_kv_cla__dense_activation_gelu__decode'

def setup_inputs():

    _w = (lambda a: (lambda t: t.to(torch.bfloat16) if t.is_floating_point() else t)(torch.as_tensor(a)))
    def _A_setup():
        B, H, Sq, Sk, D, M = 4, 32, 1, 4096, 128, 512
        Dc = 4 * D
        return {
            "x": _w((lambda *s: torch.randn(*s, dtype=torch.bfloat16) * 0.1)(B, Sq, M)),
            "c_kv": _w((lambda *s: torch.randn(*s, dtype=torch.bfloat16) * 3.2)(B, Sk, Dc)),
            "w_q": _w((lambda *s: torch.randn(*s, dtype=torch.bfloat16) * 0.8)(M, H, D) * (M ** -0.5)),
            "w_uk": _w((lambda *s: torch.randn(*s, dtype=torch.bfloat16) * 0.8)(Dc, H, D) * (Dc ** -0.5)),
            "w_uv": _w((lambda *s: torch.randn(*s, dtype=torch.bfloat16) * 3.2)(Dc, H, D) * (Dc ** -0.5)),
            "w_o": _w((lambda *s: torch.randn(*s, dtype=torch.bfloat16) * 3.2)(H, D, M) * ((H * D) ** -0.5)),
        }
    _A_in = _A_setup()

    def _B_setup():
        B, S, D, F = 4, 1, 512, 1792
        return {
            "x": _w((lambda *s: torch.randn(*s, dtype=torch.bfloat16) * 0.1)(B, S, D)),
            "w1": _w((lambda *s: torch.randn(*s, dtype=torch.bfloat16) * 3.2)(D, F) * (D ** -0.5)),
            "b1": _w((lambda *s: torch.randn(*s, dtype=torch.bfloat16) * 3.2)(F) * 0.01),
            "w2": _w((lambda *s: torch.randn(*s, dtype=torch.bfloat16) * 3.2)(F, D) * (F ** -0.5)),
            "b2": _w((lambda *s: torch.randn(*s, dtype=torch.bfloat16) * 3.2)(D) * 0.01),
        }
    _B_in = _B_setup()

    return {
        "A_x": _A_in["x"],
        "A_c_kv": _A_in["c_kv"],
        "A_w_q": _A_in["w_q"],
        "A_w_uk": _A_in["w_uk"],
        "A_w_uv": _A_in["w_uv"],
        "A_w_o": _A_in["w_o"],
        "B_x": _B_in["x"],
        "B_w1": _B_in["w1"],
        "B_b1": _B_in["b1"],
        "B_w2": _B_in["w2"],
        "B_b2": _B_in["b2"],
    }

def reference(A_x, A_c_kv, A_w_q, A_w_uk, A_w_uv, A_w_o, B_x, B_w1, B_b1, B_w2, B_b2):
    def _A_ref(x, c_kv, w_q, w_uk, w_uv, w_o):
        B, Sq, M = x.shape[0], x.shape[1], x.shape[2]
        Sk = c_kv.shape[1]
        H, D = w_q.shape[1], w_q.shape[2]
        q = torch.einsum("bqm,mhd->bhqd", x, w_q)
        k = torch.einsum("bkc,chd->bhkd", c_kv, w_uk)
        v = torch.einsum("bkc,chd->bhkd", c_kv, w_uv)
        scores = torch.einsum("bhqd,bhkd->bhqk", q, k) * (D ** -0.5)
        off = Sk - Sq
        qi = torch.arange(Sq)[:, None]
        kj = torch.arange(Sk)[None, :]
        cmask = kj <= (qi + off)
        scores = torch.where(cmask[None, None], scores, -1e9)
        p = _softmax(scores, axis=-1)
        ctx = torch.einsum("bhqk,bhkd->bhqd", p, v)
        return torch.einsum("bhqd,hdm->bqm", ctx, w_o)

    def _B_ref(x, w1, b1, w2, b2):
        h = torch.einsum("bsd,df->bsf", x, w1) + b1
        h = _gelu(h)
        return torch.einsum("bsf,fd->bsd", h, w2) + b2

    x = A_x
    xn = _layernorm(x, axis=-1)
    s = _A_ref(xn, A_c_kv, A_w_q, A_w_uk, A_w_uv, A_w_o)
    g = _silu(_B_ref(xn, B_w1, B_b1, B_w2, B_b2))
    return x + s * g
```

```cpp
#include <hip/hip_runtime.h>
#include <hip/hip_bf16.h>
#include <stdint.h>

typedef unsigned short u16;
constexpr int B = 4, H = 32, SK = 4096, D = 128, M = 512, DC = 512, F = 1792;
constexpr int NSPLIT = 64, KT = SK / NSPLIT;

__device__ __forceinline__ float bf2f(u16 v) { return __uint_as_float(((unsigned)v) << 16); }
__device__ __forceinline__ u16 f2bf(float f) { return __builtin_bit_cast(u16, (__bf16)f); }
__device__ __forceinline__ float rbf(float f) { return bf2f(f2bf(f)); }

__device__ void layernorm(const u16* x, float* xn, float* red) {
  const int tid = threadIdx.x, w = tid >> 6, lane = tid & 63;
  if (w < B) {
    float v[8], s = 0.f;
    for (int i = 0; i < 8; ++i) { v[i] = bf2f(x[w * M + lane * 8 + i]); s += v[i]; }
    for (int o = 32; o; o >>= 1) s += __shfl_xor(s, o);
    const float mean = s / M;
    float q = 0.f;
    for (int i = 0; i < 8; ++i) { float d = v[i] - mean; q += d * d; }
    for (int o = 32; o; o >>= 1) q += __shfl_xor(q, o);
    const float var = rbf(q / M), mb = rbf(mean);
    const float den = rbf(sqrtf(rbf(var + 1e-5f)));
    for (int i = 0; i < 8; ++i) xn[w * M + lane * 8 + i] = rbf(rbf(v[i] - mb) / den);
  }
  __syncthreads();
}

__global__ __launch_bounds__(256) void k_prep(const u16* x, const u16* wq, const u16* wuk, const u16* w1, const u16* b1, float* qabs, float* h1) {
  __shared__ float xn[B * M];
  __shared__ float q[B * D];
  __shared__ float part[2 * B * D];
  layernorm(x, xn, nullptr);
  const int tid = threadIdx.x;
  if (blockIdx.x < H) {
    const int h = blockIdx.x, d = tid & 127, half = tid >> 7;
    float a[B] = {0, 0, 0, 0};
    for (int m = half * 256; m < half * 256 + 256; ++m) {
      float w = bf2f(wq[(m * H + h) * D + d]);
      for (int b = 0; b < B; ++b) a[b] += xn[b * M + m] * w;
    }
    for (int b = 0; b < B; ++b) part[(half * B + b) * D + d] = a[b];
    __syncthreads();
    for (int i = tid; i < B * D; i += 256) q[i] = rbf(part[i] + part[B * D + i]);
    __syncthreads();
    for (int c = tid; c < DC; c += 256) {
      float s[B] = {0, 0, 0, 0};
      for (int dd = 0; dd < D; ++dd) {
        float w = bf2f(wuk[(c * H + h) * D + dd]);
        for (int b = 0; b < B; ++b) s[b] += q[b * D + dd] * w;
      }
      for (int b = 0; b < B; ++b) qabs[(b * H + h) * DC + c] = s[b];
    }
  } else {
    const int f = (blockIdx.x - H) * 256 + tid;
    if (f < F) {
      float a[B] = {0, 0, 0, 0};
      for (int m = 0; m < M; ++m) {
        float w = bf2f(w1[m * F + f]);
        for (int b = 0; b < B; ++b) a[b] += xn[b * M + m] * w;
      }
      for (int b = 0; b < B; ++b) {
        float t = rbf(rbf(a[b]) + bf2f(b1[f]));
        float g = 0.5f * t * (1.f + tanhf(0.7978845608028654f * (t + 0.044715f * t * t * t)));
        h1[b * F + f] = rbf(g);
      }
    }
  }
}

__global__ __launch_bounds__(256) void k_attn(const u16* ckv, const float* qabs, float* pml, float* pctx) {
  __shared__ u16 kv[KT * (DC + 8)];
  __shared__ float sc[H * KT];
  const int b = blockIdx.x / NSPLIT, sp = blockIdx.x % NSPLIT, tid = threadIdx.x;
  for (int i = tid; i < KT * DC / 8; i += 256) {
    int r = i / (DC / 8), c = i % (DC / 8);
    *(uint4*)&kv[r * (DC + 8) + c * 8] = *(const uint4*)&ckv[((size_t)b * SK + sp * KT + r) * DC + c * 8];
  }
  __syncthreads();
  for (int i = tid; i < H * KT; i += 256) {
    int h = i / KT, k = i % KT;
    float s = 0.f;
    const float* qa = qabs + (b * H + h) * DC;
    for (int c = 0; c < DC; ++c) s += qa[c] * bf2f(kv[k * (DC + 8) + c]);
    sc[i] = s * 0.08838834764831845f;
  }
  __syncthreads();
  if (tid < H) {
    float m = -INFINITY;
    for (int k = 0; k < KT; ++k) m = fmaxf(m, sc[tid * KT + k]);
    float l = 0.f;
    for (int k = 0; k < KT; ++k) { float p = __expf(sc[tid * KT + k] - m); sc[tid * KT + k] = p; l += p; }
    pml[((b * H + tid) * NSPLIT + sp) * 2] = m;
    pml[((b * H + tid) * NSPLIT + sp) * 2 + 1] = l;
  }
  __syncthreads();
  for (int i = tid; i < H * DC; i += 256) {
    int h = i / DC, c = i % DC;
    float s = 0.f;
    for (int k = 0; k < KT; ++k) s += sc[h * KT + k] * bf2f(kv[k * (DC + 8) + c]);
    pctx[((size_t)(b * H + h) * NSPLIT + sp) * DC + c] = s;
  }
}

__global__ __launch_bounds__(256) void k_comb(const float* pml, const float* pctx, const u16* wuv, const u16* wo, float* sp) {
  __shared__ float cc[B * DC];
  __shared__ float ctx[B * D];
  __shared__ float wgt[B * NSPLIT];
  const int h = blockIdx.x, tid = threadIdx.x;
  {
    int b = tid >> 6, s = tid & 63;
    float m = pml[((b * H + h) * NSPLIT + s) * 2], l = pml[((b * H + h) * NSPLIT + s) * 2 + 1];
    float mm = m;
    for (int o = 32; o; o >>= 1) mm = fmaxf(mm, __shfl_xor(mm, o));
    float e = __expf(m - mm), L = e * l;
    for (int o = 32; o; o >>= 1) L += __shfl_xor(L, o);
    wgt[tid] = e / L;
  }
  __syncthreads();
  for (int i = tid; i < B * DC; i += 256) {
    int b = i / DC, c = i % DC;
    float s = 0.f;
    for (int k = 0; k < NSPLIT; ++k) s += wgt[b * NSPLIT + k] * pctx[((size_t)(b * H + h) * NSPLIT + k) * DC + c];
    cc[i] = s;
  }
  __syncthreads();
  for (int i = tid; i < B * D; i += 256) {
    int b = i / D, d = i % D;
    float s = 0.f;
    for (int c = 0; c < DC; ++c) s += cc[b * DC + c] * bf2f(wuv[(c * H + h) * D + d]);
    ctx[i] = rbf(s);
  }
  __syncthreads();
  for (int i = tid; i < B * M; i += 256) {
    int b = i / M, m = i % M;
    float s = 0.f;
    for (int d = 0; d < D; ++d) s += ctx[b * D + d] * bf2f(wo[(h * D + d) * M + m]);
    sp[(h * B + b) * M + m] = s;
  }
}

__global__ __launch_bounds__(256) void k_final(const u16* x, const float* h1, const u16* w2, const u16* b2, const float* sp, u16* out) {
  const int i = blockIdx.x * 256 + threadIdx.x, b = i / M, m = i % M;
  float y = 0.f;
  for (int f = 0; f < F; ++f) y += h1[b * F + f] * bf2f(w2[f * M + m]);
  y = rbf(rbf(y) + bf2f(b2[m]));
  float g = rbf(y / (1.f + __expf(-y)));
  float s = 0.f;
  for (int h = 0; h < H; ++h) s += sp[(h * B + b) * M + m];
  out[i] = f2bf(bf2f(x[i]) + rbf(rbf(s) * g));
}

extern "C" void kernel_launch(void* const* d_in, const int* in_sizes, int n_in, void* d_out, int out_size, void* d_ws, size_t ws_size, hipStream_t stream) {
  const u16* x = (const u16*)d_in[0];
  const u16* ckv = (const u16*)d_in[1];
  const u16* wq = (const u16*)d_in[2];
  const u16* wuk = (const u16*)d_in[3];
  const u16* wuv = (const u16*)d_in[4];
  const u16* wo = (const u16*)d_in[5];
  const u16* w1 = (const u16*)d_in[7];
  const u16* b1 = (const u16*)d_in[8];
  const u16* w2 = (const u16*)d_in[9];
  const u16* b2 = (const u16*)d_in[10];
  float* ws = (float*)d_ws;
  float* qabs = ws;
  float* h1 = qabs + B * H * DC;
  float* pml = h1 + B * F;
  float* sp = pml + B * H * NSPLIT * 2;
  float* pctx = sp + H * B * M;
  k_prep<<<H + 7, 256, 0, stream>>>(x, wq, wuk, w1, b1, qabs, h1);
  k_attn<<<B * NSPLIT, 256, 0, stream>>>(ckv, qabs, pml, pctx);
  k_comb<<<H, 256, 0, stream>>>(pml, pctx, wuv, wo, sp);
  k_final<<<B * M / 256, 256, 0, stream>>>(x, h1, w2, b2, sp, (u16*)d_out);
}
```

```cpp
#include <hip/hip_runtime.h>
#include <hip/hip_bf16.h>
#include <stdint.h>

typedef unsigned short u16;
constexpr int B = 4, H = 32, SK = 4096, D = 128, M = 512, DC = 512, F = 1792;
constexpr int NSPLIT = 64, KT = SK / NSPLIT;

__device__ __forceinline__ float bf2f(u16 v) { return __uint_as_float(((unsigned)v) << 16); }
__device__ __forceinline__ u16 f2bf(float f) { return __builtin_bit_cast(u16, (__bf16)f); }
__device__ __forceinline__ float rbf(float f) { return bf2f(f2bf(f)); }

__device__ void layernorm(const u16* x, float* xn, float* red) {
  const int tid = threadIdx.x, w = tid >> 6, lane = tid & 63;
  if (w < B) {
    float v[8], s = 0.f;
    for (int i = 0; i < 8; ++i) { v[i] = bf2f(x[w * M + lane * 8 + i]); s += v[i]; }
    for (int o = 32; o; o >>= 1) s += __shfl_xor(s, o);
    const float mean = s / M;
    float q = 0.f;
    for (int i = 0; i < 8; ++i) { float d = v[i] - mean; q += d * d; }
    for (int o = 32; o; o >>= 1) q += __shfl_xor(q, o);
    const float var = rbf(q / M), mb = rbf(mean);
    const float den = rbf(sqrtf(rbf(var + 1e-5f)));
    for (int i = 0; i < 8; ++i) xn[w * M + lane * 8 + i] = rbf(rbf(v[i] - mb) / den);
  }
  __syncthreads();
}

__global__ __launch_bounds__(256) void k_prep(const u16* x, const u16* wq, const u16* wuk, const u16* w1, const u16* b1, float* qabs, float* h1) {
  __shared__ float xn[B * M];
  __shared__ float q[B * D];
  __shared__ float part[2 * B * D];
  layernorm(x, xn, nullptr);
  const int tid = threadIdx.x;
  if (blockIdx.x < H) {
    const int h = blockIdx.x, d = tid & 127, half = tid >> 7;
    float a[B] = {0, 0, 0, 0};
    for (int m = half * 256; m < half * 256 + 256; ++m) {
      float w = bf2f(wq[(m * H + h) * D + d]);
      for (int b = 0; b < B; ++b) a[b] += xn[b * M + m] * w;
    }
    for (int b = 0; b < B; ++b) part[(half * B + b) * D + d] = a[b];
    __syncthreads();
    for (int i = tid; i < B * D; i += 256) q[i] = rbf(part[i] + part[B * D + i]);
    __syncthreads();
    for (int c = tid; c < DC; c += 256) {
      float s[B] = {0, 0, 0, 0};
      for (int dd = 0; dd < D; ++dd) {
        float w = bf2f(wuk[(c * H + h) * D + dd]);
        for (int b = 0; b < B; ++b) s[b] += q[b * D + dd] * w;
      }
      for (int b = 0; b < B; ++b) qabs[(b * H + h) * DC + c] = s[b];
    }
  } else {
    const int f = (blockIdx.x - H) * 256 + tid;
    if (f < F) {
      float a[B] = {0, 0, 0, 0};
      for (int m = 0; m < M; ++m) {
        float w = bf2f(w1[m * F + f]);
        for (int b = 0; b < B; ++b) a[b] += xn[b * M + m] * w;
      }
      for (int b = 0; b < B; ++b) {
        float t = rbf(rbf(a[b]) + bf2f(b1[f]));
        float g = 0.5f * t * (1.f + tanhf(0.7978845608028654f * (t + 0.044715f * t * t * t)));
        h1[b * F + f] = rbf(g);
      }
    }
  }
}

__global__ __launch_bounds__(256) void k_attn(const u16* ckv, const float* qabs, float* pml, float* pctx) {
  __shared__ u16 kv[KT * (DC + 8)];
  __shared__ float sc[H * KT];
  const int b = blockIdx.x / NSPLIT, sp = blockIdx.x % NSPLIT, tid = threadIdx.x;
  for (int i = tid; i < KT * DC / 8; i += 256) {
    int r = i / (DC / 8), c = i % (DC / 8);
    *(uint4*)&kv[r * (DC + 8) + c * 8] = *(const uint4*)&ckv[((size_t)b * SK + sp * KT + r) * DC + c * 8];
  }
  __syncthreads();
  for (int i = tid; i < H * KT; i += 256) {
    int h = i / KT, k = i % KT;
    float s = 0.f;
    const float* qa = qabs + (b * H + h) * DC;
    for (int c = 0; c < DC; ++c) s += qa[c] * bf2f(kv[k * (DC + 8) + c]);
    sc[i] = s * 0.08838834764831845f;
  }
  __syncthreads();
  if (tid < H) {
    float m = -INFINITY;
    for (int k = 0; k < KT; ++k) m = fmaxf(m, sc[tid * KT + k]);
    float l = 0.f;
    for (int k = 0; k < KT; ++k) { float p = __expf(sc[tid * KT + k] - m); sc[tid * KT + k] = p; l += p; }
    pml[((b * H + tid) * NSPLIT + sp) * 2] = m;
    pml[((b * H + tid) * NSPLIT + sp) * 2 + 1] = l;
  }
  __syncthreads();
  for (int i = tid; i < H * DC; i += 256) {
    int h = i / DC, c = i % DC;
    float s = 0.f;
    for (int k = 0; k < KT; ++k) s += sc[h * KT + k] * bf2f(kv[k * (DC + 8) + c]);
    pctx[((size_t)(b * H + h) * NSPLIT + sp) * DC + c] = s;
  }
}

__global__ __launch_bounds__(256) void k_comb(const float* pml, const float* pctx, const u16* wuv, const u16* wo, float* sp) {
  __shared__ float cc[B * DC];
  __shared__ float ctx[B * D];
  __shared__ float wgt[B * NSPLIT];
  const int h = blockIdx.x, tid = threadIdx.x;
  {
    int b = tid >> 6, s = tid & 63;
    float m = pml[((b * H + h) * NSPLIT + s) * 2], l = pml[((b * H + h) * NSPLIT + s) * 2 + 1];
    float mm = m;
    for (int o = 32; o; o >>= 1) mm = fmaxf(mm, __shfl_xor(mm, o));
    float e = __expf(m - mm), L = e * l;
    for (int o = 32; o; o >>= 1) L += __shfl_xor(L, o);
    wgt[tid] = e / L;
  }
  __syncthreads();
  for (int i = tid; i < B * DC; i += 256) {
    int b = i / DC, c = i % DC;
    float s = 0.f;
    for (int k = 0; k < NSPLIT; ++k) s += wgt[b * NSPLIT + k] * pctx[((size_t)(b * H + h) * NSPLIT + k) * DC + c];
    cc[i] = s;
  }
  __syncthreads();
  for (int i = tid; i < B * D; i += 256) {
    int b = i / D, d = i % D;
    float s = 0.f;
    for (int c = 0; c < DC; ++c) s += cc[b * DC + c] * bf2f(wuv[(c * H + h) * D + d]);
    ctx[i] = rbf(s);
  }
  __syncthreads();
  for (int i = tid; i < B * M; i += 256) {
    int b = i / M, m = i % M;
    float s = 0.f;
    for (int d = 0; d < D; ++d) s += ctx[b * D + d] * bf2f(wo[(h * D + d) * M + m]);
    sp[(h * B + b) * M + m] = s;
  }
}

__global__ __launch_bounds__(256) void k_final(const u16* x, const float* h1, const u16* w2, const u16* b2, const float* sp, u16* out) {
  const int i = blockIdx.x * 256 + threadIdx.x, b = i / M, m = i % M;
  float y = 0.f;
  for (int f = 0; f < F; ++f) y += h1[b * F + f] * bf2f(w2[f * M + m]);
  y = rbf(rbf(y) + bf2f(b2[m]));
  float g = rbf(y / (1.f + __expf(-y)));
  float s = 0.f;
  for (int h = 0; h < H; ++h) s += sp[(h * B + b) * M + m];
  out[i] = f2bf(bf2f(x[i]) + rbf(rbf(s) * g));
}


typedef unsigned v4u __attribute__((ext_vector_type(4)));
template <int NT>
__global__ __launch_bounds__(512) void k_probe(const v4u* p, unsigned* sink) {
  const int t = blockIdx.x * 512 + threadIdx.x;
  v4u s = {0, 0, 0, 0};
  for (int o = 0; o < 4; ++o) {
    v4u r[8];
#pragma unroll
    for (int i = 0; i < 8; ++i) r[i] = NT ? __builtin_nontemporal_load(p + t + (o * 8 + i) * 131072) : p[t + (o * 8 + i) * 131072];
#pragma unroll
    for (int i = 0; i < 8; ++i) s ^= r[i];
  }
  if ((s.x ^ s.y ^ s.z ^ s.w) == 0x12345679u) sink[0] = 1;
}
extern "C" void kernel_launch(void* const* d_in, const int* in_sizes, int n_in, void* d_out, int out_size, void* d_ws, size_t ws_size, hipStream_t stream) {
  const u16* x = (const u16*)d_in[0];
  const u16* ckv = (const u16*)d_in[1];
  const u16* wq = (const u16*)d_in[2];
  const u16* wuk = (const u16*)d_in[3];
  const u16* wuv = (const u16*)d_in[4];
  const u16* wo = (const u16*)d_in[5];
  const u16* w1 = (const u16*)d_in[7];
  const u16* b1 = (const u16*)d_in[8];
  const u16* w2 = (const u16*)d_in[9];
  const u16* b2 = (const u16*)d_in[10];
  float* ws = (float*)d_ws;
  float* qabs = ws;
  float* h1 = qabs + B * H * DC;
  float* pml = h1 + B * F;
  float* sp = pml + B * H * NSPLIT * 2;
  float* pctx = sp + H * B * M;
  k_probe<1><<<256, 512, 0, stream>>>((const v4u*)d_ws + 6 * 1024 * 1024, (unsigned*)d_ws + 60 * 1024 * 1024);
  k_prep<<<H + 7, 256, 0, stream>>>(x, wq, wuk, w1, b1, qabs, h1);
  k_attn<<<B * NSPLIT, 256, 0, stream>>>(ckv, qabs, pml, pctx);
  k_comb<<<H, 256, 0, stream>>>(pml, pctx, wuv, wo, sp);
  k_final<<<B * M / 256, 256, 0, stream>>>(x, h1, w2, b2, sp, (u16*)d_out);
}
```

```cpp
#include <hip/hip_runtime.h>
#include <hip/hip_bf16.h>
#include <stdint.h>

typedef unsigned short u16;
constexpr int B = 4, H = 32, SK = 4096, D = 128, M = 512, DC = 512, F = 1792;
constexpr int NSPLIT = 64, KT = SK / NSPLIT;

__device__ __forceinline__ float bf2f(u16 v) { return __uint_as_float(((unsigned)v) << 16); }
__device__ __forceinline__ u16 f2bf(float f) { return __builtin_bit_cast(u16, (__bf16)f); }
__device__ __forceinline__ float rbf(float f) { return bf2f(f2bf(f)); }

__device__ void layernorm(const u16* x, float* xn, float* red) {
  const int tid = threadIdx.x, w = tid >> 6, lane = tid & 63;
  if (w < B) {
    float v[8], s = 0.f;
    for (int i = 0; i < 8; ++i) { v[i] = bf2f(x[w * M + lane * 8 + i]); s += v[i]; }
    for (int o = 32; o; o >>= 1) s += __shfl_xor(s, o);
    const float mean = s / M;
    float q = 0.f;
    for (int i = 0; i < 8; ++i) { float d = v[i] - mean; q += d * d; }
    for (int o = 32; o; o >>= 1) q += __shfl_xor(q, o);
    const float var = rbf(q / M), mb = rbf(mean);
    const float den = rbf(sqrtf(rbf(var + 1e-5f)));
    for (int i = 0; i < 8; ++i) xn[w * M + lane * 8 + i] = rbf(rbf(v[i] - mb) / den);
  }
  __syncthreads();
}

__global__ __launch_bounds__(256) void k_prep(const u16* x, const u16* wq, const u16* wuk, const u16* w1, const u16* b1, float* qabs, float* h1) {
  __shared__ float xn[B * M];
  __shared__ float q[B * D];
  __shared__ float part[2 * B * D];
  layernorm(x, xn, nullptr);
  const int tid = threadIdx.x;
  if (blockIdx.x < H) {
    const int h = blockIdx.x, d = tid & 127, half = tid >> 7;
    float a[B] = {0, 0, 0, 0};
    for (int m = half * 256; m < half * 256 + 256; ++m) {
      float w = bf2f(wq[(m * H + h) * D + d]);
      for (int b = 0; b < B; ++b) a[b] += xn[b * M + m] * w;
    }
    for (int b = 0; b < B; ++b) part[(half * B + b) * D + d] = a[b];
    __syncthreads();
    for (int i = tid; i < B * D; i += 256) q[i] = rbf(part[i] + part[B * D + i]);
    __syncthreads();
    for (int c = tid; c < DC; c += 256) {
      float s[B] = {0, 0, 0, 0};
      for (int dd = 0; dd < D; ++dd) {
        float w = bf2f(wuk[(c * H + h) * D + dd]);
        for (int b = 0; b < B; ++b) s[b] += q[b * D + dd] * w;
      }
      for (int b = 0; b < B; ++b) qabs[(b * H + h) * DC + c] = s[b];
    }
  } else {
    const int f = (blockIdx.x - H) * 256 + tid;
    if (f < F) {
      float a[B] = {0, 0, 0, 0};
      for (int m = 0; m < M; ++m) {
        float w = bf2f(w1[m * F + f]);
        for (int b = 0; b < B; ++b) a[b] += xn[b * M + m] * w;
      }
      for (int b = 0; b < B; ++b) {
        float t = rbf(rbf(a[b]) + bf2f(b1[f]));
        float g = 0.5f * t * (1.f + tanhf(0.7978845608028654f * (t + 0.044715f * t * t * t)));
        h1[b * F + f] = rbf(g);
      }
    }
  }
}

__global__ __launch_bounds__(256) void k_attn(const u16* ckv, const float* qabs, float* pml, float* pctx) {
  __shared__ u16 kv[KT * (DC + 8)];
  __shared__ float sc[H * KT];
  const int b = blockIdx.x / NSPLIT, sp = blockIdx.x % NSPLIT, tid = threadIdx.x;
  for (int i = tid; i < KT * DC / 8; i += 256) {
    int r = i / (DC / 8), c = i % (DC / 8);
    *(uint4*)&kv[r * (DC + 8) + c * 8] = *(const uint4*)&ckv[((size_t)b * SK + sp * KT + r) * DC + c * 8];
  }
  __syncthreads();
  for (int i = tid; i < H * KT; i += 256) {
    int h = i / KT, k = i % KT;
    float s = 0.f;
    const float* qa = qabs + (b * H + h) * DC;
    for (int c = 0; c < DC; ++c) s += qa[c] * bf2f(kv[k * (DC + 8) + c]);
    sc[i] = s * 0.08838834764831845f;
  }
  __syncthreads();
  if (tid < H) {
    float m = -INFINITY;
    for (int k = 0; k < KT; ++k) m = fmaxf(m, sc[tid * KT + k]);
    float l = 0.f;
    for (int k = 0; k < KT; ++k) { float p = __expf(sc[tid * KT + k] - m); sc[tid * KT + k] = p; l += p; }
    pml[((b * H + tid) * NSPLIT + sp) * 2] = m;
    pml[((b * H + tid) * NSPLIT + sp) * 2 + 1] = l;
  }
  __syncthreads();
  for (int i = tid; i < H * DC; i += 256) {
    int h = i / DC, c = i % DC;
    float s = 0.f;
    for (int k = 0; k < KT; ++k) s += sc[h * KT + k] * bf2f(kv[k * (DC + 8) + c]);
    pctx[((size_t)(b * H + h) * NSPLIT + sp) * DC + c] = s;
  }
}

__global__ __launch_bounds__(256) void k_comb(const float* pml, const float* pctx, const u16* wuv, const u16* wo, float* sp) {
  __shared__ float cc[B * DC];
  __shared__ float ctx[B * D];
  __shared__ float wgt[B * NSPLIT];
  const int h = blockIdx.x, tid = threadIdx.x;
  {
    int b = tid >> 6, s = tid & 63;
    float m = pml[((b * H + h) * NSPLIT + s) * 2], l = pml[((b * H + h) * NSPLIT + s) * 2 + 1];
    float mm = m;
    for (int o = 32; o; o >>= 1) mm = fmaxf(mm, __shfl_xor(mm, o));
    float e = __expf(m - mm), L = e * l;
    for (int o = 32; o; o >>= 1) L += __shfl_xor(L, o);
    wgt[tid] = e / L;
  }
  __syncthreads();
  for (int i = tid; i < B * DC; i += 256) {
    int b = i / DC, c = i % DC;
    float s = 0.f;
    for (int k = 0; k < NSPLIT; ++k) s += wgt[b * NSPLIT + k] * pctx[((size_t)(b * H + h) * NSPLIT + k) * DC + c];
    cc[i] = s;
  }
  __syncthreads();
  for (int i = tid; i < B * D; i += 256) {
    int b = i / D, d = i % D;
    float s = 0.f;
    for (int c = 0; c < DC; ++c) s += cc[b * DC + c] * bf2f(wuv[(c * H + h) * D + d]);
    ctx[i] = rbf(s);
  }
  __syncthreads();
  for (int i = tid; i < B * M; i += 256) {
    int b = i / M, m = i % M;
    float s = 0.f;
    for (int d = 0; d < D; ++d) s += ctx[b * D + d] * bf2f(wo[(h * D + d) * M + m]);
    sp[(h * B + b) * M + m] = s;
  }
}

__global__ __launch_bounds__(256) void k_final(const u16* x, const float* h1, const u16* w2, const u16* b2, const float* sp, u16* out) {
  const int i = blockIdx.x * 256 + threadIdx.x, b = i / M, m = i % M;
  float y = 0.f;
  for (int f = 0; f < F; ++f) y += h1[b * F + f] * bf2f(w2[f * M + m]);
  y = rbf(rbf(y) + bf2f(b2[m]));
  float g = rbf(y / (1.f + __expf(-y)));
  float s = 0.f;
  for (int h = 0; h < H; ++h) s += sp[(h * B + b) * M + m];
  out[i] = f2bf(bf2f(x[i]) + rbf(rbf(s) * g));
}


__global__ __launch_bounds__(256) void k_probe(unsigned* sink) { if (threadIdx.x == 0) sink[blockIdx.x * 32] = 1; }
extern "C" void kernel_launch(void* const* d_in, const int* in_sizes, int n_in, void* d_out, int out_size, void* d_ws, size_t ws_size, hipStream_t stream) {
  const u16* x = (const u16*)d_in[0];
  const u16* ckv = (const u16*)d_in[1];
  const u16* wq = (const u16*)d_in[2];
  const u16* wuk = (const u16*)d_in[3];
  const u16* wuv = (const u16*)d_in[4];
  const u16* wo = (const u16*)d_in[5];
  const u16* w1 = (const u16*)d_in[7];
  const u16* b1 = (const u16*)d_in[8];
  const u16* w2 = (const u16*)d_in[9];
  const u16* b2 = (const u16*)d_in[10];
  float* ws = (float*)d_ws;
  float* qabs = ws;
  float* h1 = qabs + B * H * DC;
  float* pml = h1 + B * F;
  float* sp = pml + B * H * NSPLIT * 2;
  float* pctx = sp + H * B * M;
  for (int i = 0; i < 30; ++i) k_probe<<<256, 256, 0, stream>>>((unsigned*)d_ws + 60 * 1024 * 1024);
  k_prep<<<H + 7, 256, 0, stream>>>(x, wq, wuk, w1, b1, qabs, h1);
  k_attn<<<B * NSPLIT, 256, 0, stream>>>(ckv, qabs, pml, pctx);
  k_comb<<<H, 256, 0, stream>>>(pml, pctx, wuv, wo, sp);
  k_final<<<B * M / 256, 256, 0, stream>>>(x, h1, w2, b2, sp, (u16*)d_out);
}
```

```cpp
#include <hip/hip_runtime.h>
#include <hip/hip_bf16.h>
#include <stdint.h>

typedef unsigned u32;
typedef unsigned short u16;
typedef u32 u32x2 __attribute__((ext_vector_type(2)));
typedef u32 u32x4 __attribute__((ext_vector_type(4)));
typedef float f32x2 __attribute__((ext_vector_type(2)));
typedef float f32x4 __attribute__((ext_vector_type(4)));
typedef __bf16 bf16x2 __attribute__((ext_vector_type(2)));
typedef __bf16 bf16x8 __attribute__((ext_vector_type(8)));
typedef short s16x4 __attribute__((ext_vector_type(4)));
#define LAS __attribute__((address_space(3)))
#define DI __device__ __forceinline__

#define P_NA 0
#define P_N0 2
#define P_N1 6
#define P_D3 20
#define P_D4 12
#define P_D5 12
#define P_LATE 0
constexpr int NA = P_NA, N0 = P_N0, N1 = P_N1, NR = 16 - NA - N0 - N1;
constexpr u32 SPIN_MAX = 1u << 16;
constexpr u32 SENT = 0xFFFFFFFFu;

constexpr u32 W_H1 = 0x1000, W_H2 = W_H1 + 0x80000, W_ST = W_H2 + 0x40000, W_HF = W_ST + 0x10000;
constexpr u32 W_H4 = W_HF + 0x40000, W_H5 = W_H4 + 0x40000, W_HY = W_H5 + 0x40000, W_H3 = W_HY + 0x40000;
constexpr u32 W_DG = W_H3 + 0x400000;

constexpr u32 L_X = 131072, L_Y = L_X + 16384, L_TOTAL = L_Y + 16384;
constexpr u32 L_Q0 = 15 * 1024, L_Q1 = 31 * 1024, L_XN = 47 * 1024;
constexpr u32 L_W1 = L_X, L_MX = L_X + 8192, L_P = L_X + 9216, L_CB = L_X + 13312, L_CX = L_X + 14336, L_HB = L_X + 15360;
constexpr u32 L_WO = 0, L_W2 = 16384;

struct Args {
  const char *x, *ckv, *wq, *wuk, *wuv, *wo, *w1, *b1, *w2, *b2;
  char *out, *ws;
};

#define WAITV(n) asm volatile("s_waitcnt vmcnt(%0)" ::"i"(n) : "memory")
#define BARRIER() asm volatile("s_waitcnt lgkmcnt(0)\n\ts_barrier" ::: "memory")
#define PIN(v) asm volatile("" : "+v"(v))

template <int NT> DI void dma(const char* base, u32 voff, u32 lds) {
  u32 keep;
  if (NT)
    asm volatile("s_mov_b32 %0, m0\n\ts_mov_b32 m0, %3\n\ts_nop 0\n\tglobal_load_lds_dwordx4 %1, %2 nt\n\ts_mov_b32 m0, %0"
                 : "=&s"(keep) : "v"(voff), "s"(base), "s"(lds) : "memory");
  else
    asm volatile("s_mov_b32 %0, m0\n\ts_mov_b32 m0, %3\n\ts_nop 0\n\tglobal_load_lds_dwordx4 %1, %2\n\ts_mov_b32 m0, %0"
                 : "=&s"(keep) : "v"(voff), "s"(base), "s"(lds) : "memory");
}
template <int OFF> DI void ldc4(u32x4& d, const char* base, u32 voff) {
  asm volatile("global_load_dwordx4 %0, %1, %2 offset:%3 sc1" : "=&v"(d) : "v"(voff), "s"(base), "i"(OFF) : "memory");
}
template <int OFF> DI void ldc2(u32x2& d, const char* base, u32 voff) {
  asm volatile("global_load_dwordx2 %0, %1, %2 offset:%3 sc1" : "=&v"(d) : "v"(voff), "s"(base), "i"(OFF) : "memory");
}
DI void ldg4(u32x4& d, const char* base, u32 voff) {
  asm volatile("global_load_dwordx4 %0, %1, %2" : "=&v"(d) : "v"(voff), "s"(base) : "memory");
}
DI void ldg2(u32x2& d, const char* base, u32 voff) {
  asm volatile("global_load_dwordx2 %0, %1, %2" : "=&v"(d) : "v"(voff), "s"(base) : "memory");
}
DI void ldgh(u32& d, const char* base, u32 voff) {
  asm volatile("global_load_ushort %0, %1, %2" : "=&v"(d) : "v"(voff), "s"(base) : "memory");
}
DI void stc4(const char* base, u32 voff, u32x4 v) {
  asm volatile("global_store_dwordx4 %0, %1, %2 sc1\n\ts_nop 1" ::"v"(voff), "v"(v), "s"(base) : "memory");
}
DI void stc2(const char* base, u32 voff, u32x2 v) {
  asm volatile("global_store_dwordx2 %0, %1, %2 sc1" ::"v"(voff), "v"(v), "s"(base) : "memory");
}
DI void stc1(const char* base, u32 voff, u32 v) {
  asm volatile("global_store_dword %0, %1, %2 sc1" ::"v"(voff), "v"(v), "s"(base) : "memory");
}

DI u32x4 lr4(u32 a) { return *(LAS u32x4*)a; }
DI void lw4(u32 a, u32x4 v) { *(LAS u32x4*)a = v; }
DI void lw2(u32 a, u32x2 v) { *(LAS u32x2*)a = v; }
DI void lw1(u32 a, u32 v) { *(LAS u32*)a = v; }
DI u32x4 ltr(u32 a0, u32 a1) {
  u32x2 p = __builtin_bit_cast(u32x2, __builtin_amdgcn_ds_read_tr16_b64_v4i16((LAS s16x4*)a0));
  u32x2 q = __builtin_bit_cast(u32x2, __builtin_amdgcn_ds_read_tr16_b64_v4i16((LAS s16x4*)a1));
  return u32x4{p.x, p.y, q.x, q.y};
}
DI f32x4 mfma(u32x4 a, u32x4 b, f32x4 c) {
  return __builtin_amdgcn_mfma_f32_16x16x32_bf16(__builtin_bit_cast(bf16x8, a), __builtin_bit_cast(bf16x8, b), c, 0, 0, 0);
}
DI u32 fu(float f) { return __float_as_uint(f); }
DI float uf(u32 u) { return __uint_as_float(u); }
DI u32 pk(float a, float b) { return __builtin_bit_cast(u32, __builtin_convertvector(f32x2{a, b}, bf16x2)); }
DI float lo(u32 u) { return uf(u << 16); }
DI float hi(u32 u) { return uf(u & 0xffff0000u); }
DI float rbf(float a) { return lo(pk(a, a)); }
DI u32 canon(u32 u) { return u < SENT - 1 ? u : SENT - 1; }
DI u32x4 canon4(f32x4 v) { return u32x4{canon(fu(v.x)), canon(fu(v.y)), canon(fu(v.z)), canon(fu(v.w))}; }
DI u32 umax(u32 a, u32 b) { return a > b ? a : b; }
DI u32 umax4(u32x4 v) { return umax(umax(v.x, v.y), umax(v.z, v.w)); }
template <int C> DI float ror(float v) { return uf((u32)__builtin_amdgcn_update_dpp(0, (int)fu(v), 0x120 + C, 0xf, 0xf, true)); }
template <int BANKS> DI u32 ror8m(u32 keep, u32 src) { return (u32)__builtin_amdgcn_update_dpp((int)keep, (int)src, 0x128, 0xf, BANKS, false); }
DI float add32(float a, float b) { auto s = __builtin_amdgcn_permlane32_swap(fu(a), fu(b), false, false); return uf(s[0]) + uf(s[1]); }
DI float add16(float a, float b) { auto s = __builtin_amdgcn_permlane16_swap(fu(a), fu(b), false, false); return uf(s[0]) + uf(s[1]); }
DI float max32(float a) { auto s = __builtin_amdgcn_permlane32_swap(fu(a), fu(a), false, false); return fmaxf(uf(s[0]), uf(s[1])); }
DI float max16(float a) { auto s = __builtin_amdgcn_permlane16_swap(fu(a), fu(a), false, false); return fmaxf(uf(s[0]), uf(s[1])); }
DI float wsum(float v) {
  v += ror<8>(v); v += ror<4>(v); v += ror<2>(v); v += ror<1>(v);
  v = add16(v, v);
  return add32(v, v);
}
DI bool none(bool bad) { return __builtin_amdgcn_ballot_w64(bad) == 0; }
DI float ex2(float v) { return __builtin_amdgcn_exp2f(v); }
DI float rcp(float v) { return __builtin_amdgcn_rcpf(v); }
template <int N> DI void nap() { if (N > 0) __builtin_amdgcn_s_sleep(N); }

DI void red8(const float (&a)[8], float& o0, float& o1) {
  float r0 = add32(a[0], a[4]), r1 = add32(a[1], a[5]), r2 = add32(a[2], a[6]), r3 = add32(a[3], a[7]);
  float s0 = add16(r0, r2), s1 = add16(r1, r3);
  o0 = s0 + ror<8>(s0);
  o1 = s1 + ror<8>(s1);
}
DI float red4(f32x4 a) {
  float r0 = add32(a.x, a.z), r1 = add32(a.y, a.w);
  float s = add16(r0, r1);
  return s + ror<8>(s);
}

#define STAMP(i)

__global__ __launch_bounds__(512, 2) void mla_ffn_decode(Args a) {
  const u32 tid = threadIdx.x, bid = blockIdx.x, lane = tid & 63;
  const u32 w = __builtin_amdgcn_readfirstlane(tid >> 6);
  STAMP(0);
  const u32 xcd = bid & 7, r = bid >> 3, grp = r & 1, j = (r >> 1) & 7, h = grp * 16 + xcd * 2 + (r >> 4);
  const u32 ab = xcd >> 1, aks = (xcd & 1) * 16 + (r >> 1), hg = 1 - grp, sp = ab * 32 + aks;
  const u32 mq = j >> 1, dh = j & 1;
  const u32 l15 = lane & 15, g = lane >> 4, bq = lane & 3, u = (lane >> 2) & 3, l7 = lane & 7, l3 = lane >> 3;
  const u32 tq = (lane >> 2) & 3, tp = lane & 3;
  const bool ffn = w == 6 && h < 28, fin = w == 7 && h >= 24;
  const char* ws = a.ws;
  const u32x4 sent4 = {SENT, SENT, SENT, SENT};
  u32 spins = 0;

  const u32 o5 = (j * 8 + bq * 2 + (u >> 1)) * 4096 + h * 128 + (u & 1) * 64 + g * 16;
  const u32 ohf = W_HF + h * 8192 + j * 1024 + lane * 16;
  const u32 ost = W_ST + (hg * 16 + l15) * 2048 + sp * 16;
  const u32 o3 = W_H3 + (((hg * 16 + l7) * 8 + w) * 128 + sp) * 128 + (((lane >> 3) & 1) * 4 + g) * 16;

  u32x4 xv;
  if (w < 4) {
    ldg4(xv, a.x, w * 1024 + lane * 16);
  } else {
    if (w == 7 && lane < 16) stc2(ws, ost, u32x2{SENT, SENT});
    const u32 oc = w == 4 ? ohf : w == 5 ? W_H4 + h * 8192 + l3 * 1024 + j * 128 + l7 * 16 : w == 6 ? W_H5 + o5 : W_HY + o5;
    stc4(ws, oc, sent4);
  }
  {
    const u32 f = ((l3 >> 1) & 1);
    const u32 vq = ((mq * 128 + w * 16 + l3) * 32 + h) * 256 + dh * 128;
    dma<1>(a.wq, vq + ((l7 ^ (f << 1)) << 4), L_X + w * 2048);
    dma<1>(a.wq, vq + 8 * 8192 + ((l7 ^ ((f | 2) << 1)) << 4), L_X + w * 2048 + 1024);
#pragma unroll
    for (int i = 0; i < 2; ++i) {
      const u32 row = w * 8 + i * 4 + g;
      dma<1>(a.wuk, ((j * 64 + row) * 32 + h) * 256 + ((l15 ^ (row & 15)) << 4), L_Y + w * 2048 + i * 1024);
    }
  }
  stc4(ws, o3, sent4);
  stc4(ws, o3 + 8 * 8 * 16384, sent4);
  const char* kvb = a.ckv + (size_t)(ab * 4096 + aks * 128 + w * 16) * 1024;
  const u32 lkv = w * 16384;
#define KVROWS(A, B) _Pragma("unroll") for (int i = (A); i < (B); ++i) dma<0>(kvb + i * 1024, (lane ^ ((i & 7) << 1)) << 4, lkv + i * 1024)
  KVROWS(0, NA);
  const u32 tag = *(const __attribute__((address_space(4))) u32*)ws + 1;

  if (w < 4) {
    WAITV(6 + NA);
    PIN(xv);
    float v[8] = {lo(xv.x), hi(xv.x), lo(xv.y), hi(xv.y), lo(xv.z), hi(xv.z), lo(xv.w), hi(xv.w)};
    float s = 0.f;
#pragma unroll
    for (int i = 0; i < 8; ++i) s += v[i];
    const float mean = wsum(s) * (1.f / 512.f);
    float q2 = 0.f;
#pragma unroll
    for (int i = 0; i < 8; ++i) { const float d = v[i] - mean; q2 += d * d; }
    const float var = rbf(wsum(q2) * (1.f / 512.f)), mb = rbf(mean);
    const float den = rbf(sqrtf(rbf(var + 1e-5f)));
#pragma unroll
    for (int i = 0; i < 8; ++i) v[i] = rbf(v[i] - mb) / den;
    if (g == mq) lw4(L_XN + l15 * 64 + w * 16, u32x4{pk(v[0], v[1]), pk(v[2], v[3]), pk(v[4], v[5]), pk(v[6], v[7])});
  }
  WAITV(4 + NA);
  BARRIER();
  STAMP(1);

  const u32 trn = (g * 8 + tq) * 128 + (tp & 1) * 8, trf = ((tq >> 1) | ((g & 1) << 1)) << 1;
  const u32 bfr = g * 64 + bq * 16;
  u32x4 xf0, xf1;
  if (w == 6) { xf0 = lr4(L_XN + dh * 512 + bfr); xf1 = lr4(L_XN + dh * 512 + 256 + bfr); }
  if (w < 4) {
    const u32 ta = L_X + trn + (((2 * w + (tp >> 1)) ^ trf) << 4);
    f32x4 acc = {0, 0, 0, 0};
#pragma unroll
    for (int ks = 0; ks < 4; ++ks) acc = mfma(ltr(ta + ks * 4096, ta + ks * 4096 + 512), lr4(L_XN + ks * 256 + bfr), acc);
    const float v0 = (u & 1) ? acc.z : acc.x, v1 = (u & 1) ? acc.w : acc.y;
    if (u < 2) stc4(ws, W_H1 + h * 16384 + dh * 8192 + w * 2048 + mq * 512 + (g * 8 + l7) * 16, u32x4{fu(v0), tag, fu(v1), tag});
  }
  STAMP(2);
  KVROWS(NA, NA + N0);
  {
    const u32 o1 = W_H1 + h * 16384 + w * 2048 + lane * 16;
    u32x4 p0, p1;
    for (;;) {
      ldc4<0>(p0, ws, o1);
      ldc4<1024>(p1, ws, o1);
      WAITV(0);
      PIN(p0); PIN(p1);
      if (none(((p0.y ^ tag) | (p0.w ^ tag) | (p1.y ^ tag) | (p1.w ^ tag)) != 0) || ++spins > SPIN_MAX) break;
    }
    const float a0 = uf(p0.x) + uf(p1.x), a1 = uf(p0.z) + uf(p1.z);
    const u32 qv = pk(add32(a0, a0), add32(a1, a1));
    const u32 s5 = lane & 31;
    lw1((lane < 32 ? L_Q0 : L_Q1) + (2 * w + (s5 >> 4)) * 64 + bq * 16 + ((s5 >> 3) & 1) * 8 + ((s5 >> 2) & 1) * 4, qv);
  }
  BARRIER();
  STAMP(3);

  if (w < 2) {
    const u32 ua = (L_Y + (w * 32 + l15) * 256) + ((g ^ l15) << 4), qa = (w ? L_Q1 : L_Q0) + bfr;
    f32x4 c0 = {0, 0, 0, 0}, c1 = {0, 0, 0, 0};
#pragma unroll
    for (int ks = 0; ks < 4; ++ks) {
      const u32x4 bf = lr4(qa + ks * 256);
      c0 = mfma(lr4(ua ^ (ks << 6)), bf, c0);
      c1 = mfma(lr4((ua ^ (ks << 6)) + 4096), bf, c1);
    }
    const float sc = 0.08838834764831845f * 1.4426950408889634f;
    const f32x4 c = ((u & 1) ? c1 : c0) * sc;
    if (u < 2) stc4(ws, W_H2 + (bq * 32 + h) * 2048 + j * 256 + w * 128 + u * 64 + g * 16, u32x4{pk(c.x, c.y), tag, pk(c.z, c.w), tag});
  }
  STAMP(4);
  KVROWS(NA + N0, NA + N0 + N1);
  {
    const u32 o2 = W_H2 + (ab * 32 + hg * 16 + 2 * w) * 2048 + lane * 16;
    u32x4 s0, s1, s2, s3;
    ldc4<0>(s0, ws, o2); ldc4<1024>(s1, ws, o2); ldc4<2048>(s2, ws, o2); ldc4<3072>(s3, ws, o2);
    KVROWS(NA + N0 + N1, 16);
    WAITV(NR);
    for (;;) {
      PIN(s0); PIN(s1); PIN(s2); PIN(s3);
      const u32 bad = (s0.y ^ tag) | (s0.w ^ tag) | (s1.y ^ tag) | (s1.w ^ tag) | (s2.y ^ tag) | (s2.w ^ tag) | (s3.y ^ tag) | (s3.w ^ tag);
      if (none(bad != 0) || ++spins > SPIN_MAX) break;
      ldc4<0>(s0, ws, o2); ldc4<1024>(s1, ws, o2); ldc4<2048>(s2, ws, o2); ldc4<3072>(s3, ws, o2);
      WAITV(0);
    }
    const u32 h0 = (2 * w) & 7;
    const u32 i0 = L_X + 2 * w * 1024 + (((lane >> 1) ^ (h0 << 1)) << 4) + (lane & 1) * 8, i1 = (i0 ^ 32) + 1024;
    lw2(i0, u32x2{s0.x, s0.z}); lw2(i0 + 512, u32x2{s1.x, s1.z});
    lw2(i1, u32x2{s2.x, s2.z}); lw2(i1 + 512, u32x2{s3.x, s3.z});
  }
  STAMP(5);
  BARRIER();

  const u32 frag = l15 * 1024 + ((g ^ (l7 << 1)) << 4);
  u32x4 fr[16];
#pragma unroll
  for (int ks = 0; ks < 16; ++ks) fr[ks] = lr4(L_X + (frag ^ ((ks & 3) << 6)) + (ks >> 2) * 256);
#pragma unroll
  for (int i = 0; i < 2; ++i) {
    const u32 row = w * 8 + i * 4 + g;
    dma<1>(a.wuv, ((j * 64 + row) * 32 + h) * 256 + ((l15 ^ ((row & 7) << 1)) << 4), L_Y + w * 2048 + i * 1024);
  }
  BARRIER();
  STAMP(6);
  if (ffn) {
#pragma unroll
    for (int i = 0; i < 8; ++i) {
      const u32 f = ((l3 >> 1) & 1) | ((i & 1) << 1);
      dma<1>(a.w1, (j * 64 + i * 8 + l3) * 3584 + h * 128 + ((l7 ^ (f << 1)) << 4), L_W1 + i * 1024);
    }
    WAITV(10);
  } else {
    WAITV(2);
  }
  STAMP(7);

  f32x4 sc0 = {0, 0, 0, 0}, sc1 = {0, 0, 0, 0};
#pragma unroll
  for (int ks = 0; ks < 16; ks += 2) {
    sc0 = mfma(lr4(lkv + (frag ^ ((ks & 3) << 6)) + (ks >> 2) * 256), fr[ks], sc0);
    sc1 = mfma(lr4(lkv + (frag ^ (((ks + 1) & 3) << 6)) + (ks >> 2) * 256), fr[ks + 1], sc1);
  }
  const f32x4 sv = sc0 + sc1;
  {
    const float m = max32(max16(fmaxf(fmaxf(sv.x, sv.y), fmaxf(sv.z, sv.w))));
    if (g == 0) lw1(L_MX + l15 * 32 + w * 4, fu(m));
  }
  BARRIER();
  STAMP(8);
  float mx;
  {
    const u32x4 m0 = lr4(L_MX + l15 * 32), m1 = lr4(L_MX + l15 * 32 + 16);
    const u32 r8 = (g & 1) * 4 + tq;
    const u32 va = (g * 4 + tq) * 1024 + (((w * 8 + (tp >> 1)) ^ (r8 << 1)) << 4) + (tp & 1) * 8;
#pragma unroll
    for (int t = 0; t < 4; ++t)
#pragma unroll
      for (int ks = 0; ks < 4; ++ks) fr[t * 4 + ks] = ltr((va ^ (t << 5)) + ks * 32768, (va ^ (t << 5)) + ks * 32768 + 16384);
    mx = fmaxf(fmaxf(fmaxf(uf(m0.x), uf(m0.y)), fmaxf(uf(m0.z), uf(m0.w))), fmaxf(fmaxf(uf(m1.x), uf(m1.y)), fmaxf(uf(m1.z), uf(m1.w))));
    lw2(L_P + (w >> 1) * 1024 + lane * 16 + (w & 1) * 8, u32x2{pk(ex2(sv.x - mx), ex2(sv.y - mx)), pk(ex2(sv.z - mx), ex2(sv.w - mx))});
  }
  BARRIER();
  STAMP(9);
#define LATE_DMA()                                                                                              \
  do {                                                                                                          \
    const u32 f = ((l3 >> 1) & 1);                                                                              \
    const u32 vo = (h * 128 + w * 16 + l3) * 1024 + j * 128;                                                    \
    if (ffn) {                                                                                                  \
      _Pragma("unroll") for (int i = 0; i < 8; ++i)                                                             \
          dma<1>(a.w2, (h * 64 + i * 8 + l3) * 1024 + j * 128 + ((l7 ^ (l3 & 6)) << 4), L_W2 + i * 1024);       \
    }                                                                                                           \
    dma<1>(a.wo, vo + ((l7 ^ (f << 1)) << 4), L_WO + w * 2048);                                                 \
    dma<1>(a.wo, vo + 8 * 1024 + ((l7 ^ ((f | 2) << 1)) << 4), L_WO + w * 2048 + 1024);                         \
  } while (0)
  {
    f32x4 o[4] = {{0, 0, 0, 0}, {0, 0, 0, 0}, {0, 0, 0, 0}, {0, 0, 0, 0}}, ol = {0, 0, 0, 0};
    const u32x4 ones = {0x3F803F80u, 0x3F803F80u, 0x3F803F80u, 0x3F803F80u};
#pragma unroll
    for (int ks = 0; ks < 4; ++ks) {
      const u32x4 pf = lr4(L_P + ks * 1024 + lane * 16);
#pragma unroll
      for (int t = 0; t < 4; ++t) o[t] = mfma(fr[t * 4 + ks], pf, o[t]);
      if (w == 7) ol = mfma(ones, pf, ol);
    }
    if (w == 7 && lane < 16) stc2(ws, ost, u32x2{canon(fu(mx)), canon(fu(ol.x))});
    const u32x4 r0 = {pk(o[0].x, o[0].y), pk(o[0].z, o[0].w), pk(o[1].x, o[1].y), pk(o[1].z, o[1].w)};
    const u32x4 r1 = {pk(o[2].x, o[2].y), pk(o[2].z, o[2].w), pk(o[3].x, o[3].y), pk(o[3].z, o[3].w)};
    u32x4 n0, n1;
#pragma unroll
    for (int e = 0; e < 4; ++e) { n0[e] = canon(ror8m<0xC>(r0[e], r1[e])); n1[e] = canon(ror8m<0x3>(r1[e], r0[e])); }
    stc4(ws, o3, n0);
    stc4(ws, o3 + 8 * 8 * 16384, n1);
  }
  STAMP(10);
  if (P_LATE == 0) LATE_DMA();

  u32x2 b1v;
  u32 xo, b2o;
  const u32 fb = (h >> 1) & 3, fm = j * 64 + (h & 1) * 32 + l7 * 4 + (lane >> 5) * 2 + ((lane >> 4) & 1);
  if (ffn) {
    ldg2(b1v, a.b1, (h * 64 + u * 16 + g * 4) * 2);
    WAITV(P_LATE == 0 ? 13 : 3);
    f32x4 d[4];
#pragma unroll
    for (int t = 0; t < 4; ++t) {
      const u32 ta = L_W1 + trn + (((2 * t + (tp >> 1)) ^ trf) << 4);
      d[t] = mfma(ltr(ta, ta + 512), xf0, f32x4{0, 0, 0, 0});
      d[t] = mfma(ltr(ta + 4096, ta + 4096 + 512), xf1, d[t]);
    }
    stc4(ws, ohf, canon4(u == 0 ? d[0] : u == 1 ? d[1] : u == 2 ? d[2] : d[3]));
    nap<P_D3 - 6>();
  } else {
    if (fin) { ldgh(xo, a.x, (fb * 512 + fm) * 2); ldgh(b2o, a.b2, fm * 2); }
    nap<P_D3>();
  }
  {
    const u32 os = W_ST + h * 2048 + ((w >> 1) * 32 + l3) * 16, osn = os + (w & 1) * 256, oso = os + ((w & 1) ^ 1) * 256;
    const u32 od = W_H3 + ((h * 8 + j) * 128 + w * 16) * 128 + lane * 16;
    u32x2 e0, e1, e2, e3;
    u32x4 d0, d1;
    STAMP(11);
    ldc2<0>(e0, ws, osn); ldc2<128>(e1, ws, osn); ldc2<0>(e2, ws, oso); ldc2<128>(e3, ws, oso);
    ldc4<0>(d0, ws, od); ldc4<1024>(d1, ws, od);
    if (P_LATE == 1) {
      LATE_DMA();
      if (ffn) WAITV(10); else WAITV(2);
    } else {
      WAITV(0);
    }
    for (;;) {
      PIN(e0); PIN(e1); PIN(e2); PIN(e3); PIN(d0); PIN(d1);
      const u32 t = umax(umax(umax(umax(e0.x, e0.y), umax(e1.x, e1.y)), umax(umax(e2.x, e2.y), umax(e3.x, e3.y))), umax(umax4(d0), umax4(d1)));
      if (none(t == SENT) || ++spins > SPIN_MAX) break;
      ldc2<0>(e0, ws, osn); ldc2<128>(e1, ws, osn); ldc2<0>(e2, ws, oso); ldc2<128>(e3, ws, oso);
      ldc4<0>(d0, ws, od); ldc4<1024>(d1, ws, od);
      WAITV(0);
    }
    STAMP(12);
    float m = fmaxf(fmaxf(uf(e0.x), uf(e1.x)), fmaxf(uf(e2.x), uf(e3.x)));
    m = max32(max16(fmaxf(m, ror<8>(m))));
    float w0 = ex2(uf(e0.x) - m), w1 = ex2(uf(e1.x) - m);
    float l = w0 * uf(e0.y) + w1 * uf(e1.y) + ex2(uf(e2.x) - m) * uf(e2.y) + ex2(uf(e3.x) - m) * uf(e3.y);
    l += ror<8>(l);
    l = add16(l, l);
    l = add32(l, l);
    float acc[8];
#pragma unroll
    for (int e = 0; e < 4; ++e) {
      acc[2 * e] = w0 * lo(d0[e]) + w1 * lo(d1[e]);
      acc[2 * e + 1] = w0 * hi(d0[e]) + w1 * hi(d1[e]);
    }
    float c0, c1;
    red8(acc, c0, c1);
    const float il = rcp(l);
    const u32 col = (w & 1) * 4 + (w >> 1);
    if (!(lane & 8)) lw1(L_CB + l7 * 128 + ((col ^ l7) << 4) + (lane >> 5) * 8 + ((lane >> 4) & 1) * 4, pk(c0 * il, c1 * il));
  }
  BARRIER();
  STAMP(13);
  {
    const u32 r8 = (g & 1) * 4 + tq;
    const u32 va = L_Y + (g * 4 + tq) * 256 + (((2 * w + (tp >> 1)) ^ (r8 << 1)) << 4) + (tp & 1) * 8;
    f32x4 c = mfma(ltr(va, va + 4096), lr4(L_CB + g * 128 + ((l7 ^ g) << 4)), f32x4{0, 0, 0, 0});
    c = mfma(ltr(va + 8192, va + 8192 + 4096), lr4(L_CB + (4 + g) * 128 + ((l7 ^ (4 + g)) << 4)), c);
    const u32x2 v = {canon(pk(c.x + ror<4>(c.x), c.y + ror<4>(c.y))), canon(pk(c.z + ror<4>(c.z), c.w + ror<4>(c.w)))};
    if (u == 0) stc2(ws, W_H4 + h * 8192 + w * 1024 + j * 128 + bq * 32 + g * 8, v);
  }
  STAMP(14);
  float gate = 0.f;
  if (ffn) {
    const u32 of = W_HF + h * 8192 + lane * 16;
    u32x4 p[8];
    for (;;) {
      ldc4<0>(p[0], ws, of); ldc4<1024>(p[1], ws, of); ldc4<2048>(p[2], ws, of); ldc4<3072>(p[3], ws, of);
      ldc4<0>(p[4], ws, of + 4096); ldc4<1024>(p[5], ws, of + 4096); ldc4<2048>(p[6], ws, of + 4096); ldc4<3072>(p[7], ws, of + 4096);
      WAITV(0);
      u32 t = 0;
#pragma unroll
      for (int i = 0; i < 8; ++i) { PIN(p[i]); t = umax(t, umax4(p[i])); }
      if (none(t == SENT) || ++spins > SPIN_MAX) break;
    }
    PIN(b1v);
    const float bb[4] = {lo(b1v.x), hi(b1v.x), lo(b1v.y), hi(b1v.y)};
    float ge[4];
#pragma unroll
    for (int e = 0; e < 4; ++e) {
      float s = 0.f;
#pragma unroll
      for (int i = 0; i < 8; ++i) s += uf(p[i][e]);
      const float t = rbf(rbf(s) + bb[e]);
      const float z = ex2(2.302208198f * (t + 0.044715f * t * t * t));
      ge[e] = t - t * rcp(z + 1.f);
    }
    lw2(L_HB + (u >> 1) * 256 + bfr + (u & 1) * 8, u32x2{pk(ge[0], ge[1]), pk(ge[2], ge[3])});
    asm volatile("s_waitcnt lgkmcnt(0)" ::: "memory");
    const u32x4 hb0 = lr4(L_HB + bfr), hb1 = lr4(L_HB + 256 + bfr);
    const u32 f2 = (((g & 1) << 1) | (tq >> 1)) << 1;
    f32x4 d[4];
#pragma unroll
    for (int t = 0; t < 4; ++t) {
      const u32 ta = L_W2 + (g * 4 + tq) * 128 + (((2 * t + (tp >> 1)) ^ f2) << 4) + (tp & 1) * 8;
      d[t] = mfma(ltr(ta, ta + 2048), hb0, f32x4{0, 0, 0, 0});
      d[t] = mfma(ltr(ta + 4096, ta + 4096 + 2048), hb1, d[t]);
    }
    stc4(ws, W_HY + o5, canon4(u == 0 ? d[0] : u == 1 ? d[1] : u == 2 ? d[2] : d[3]));
  } else {
    nap<P_D4>();
  }
  {
    const u32 o4 = W_H4 + h * 8192 + w * 1024 + lane * 16;
    u32x4 d;
    for (;;) {
      ldc4<0>(d, ws, o4);
      WAITV(0);
      PIN(d);
      if (none(umax4(d) == SENT) || ++spins > SPIN_MAX) break;
    }
    const float acc[8] = {lo(d.x), hi(d.x), lo(d.y), hi(d.y), lo(d.z), hi(d.z), lo(d.w), hi(d.w)};
    float c0, c1;
    red8(acc, c0, c1);
    if (!(lane & 8)) lw1(L_CX + (2 * w + (lane & 1)) * 64 + (l7 >> 1) * 16 + (lane >> 5) * 8 + ((lane >> 4) & 1) * 4, pk(c0, c1));
  }
  BARRIER();
  STAMP(15);
  if (w < 2) {
    const u32 t0 = L_WO + trn + (((4 * w + (tp >> 1)) ^ trf) << 4), t1 = t0 ^ 32;
    f32x4 c0 = {0, 0, 0, 0}, c1 = {0, 0, 0, 0};
#pragma unroll
    for (int ks = 0; ks < 4; ++ks) {
      const u32x4 bf = lr4(L_CX + ks * 256 + bfr);
      c0 = mfma(ltr(t0 + ks * 4096, t0 + ks * 4096 + 512), bf, c0);
      c1 = mfma(ltr(t1 + ks * 4096, t1 + ks * 4096 + 512), bf, c1);
    }
    if (u < 2) stc4(ws, W_H5 + (j * 8 + bq * 2 + w) * 4096 + h * 128 + u * 64 + g * 16, canon4((u & 1) ? c1 : c0));
  }
  STAMP(16);
  if (fin) {
    const u32 fo = (j * 8 + (h & 7)) * 4096 + lane * 16;
    const u32 o3y = lane < 32 ? fo + 3072 : fo;
    u32x4 p0, p1, p2, p3;
    for (;;) {
      ldc4<0>(p0, ws, W_HY + fo); ldc4<1024>(p1, ws, W_HY + fo); ldc4<2048>(p2, ws, W_HY + fo); ldc4<0>(p3, ws, W_HY + o3y);
      WAITV(0);
      PIN(p0); PIN(p1); PIN(p2); PIN(p3);
      if (none(umax(umax(umax4(p0), umax4(p1)), umax(umax4(p2), umax4(p3))) == SENT) || ++spins > SPIN_MAX) break;
    }
    PIN(xo); PIN(b2o);
    {
      const f32x4 z = {0, 0, 0, 0};
      const f32x4 s = __builtin_bit_cast(f32x4, p0) + __builtin_bit_cast(f32x4, p1) + __builtin_bit_cast(f32x4, p2) + (lane < 32 ? __builtin_bit_cast(f32x4, p3) : z);
      const float y = rbf(rbf(red4(s)) + lo(b2o));
      gate = rbf(y * rcp(1.f + ex2(-1.4426950408889634f * y)));
    }
    STAMP(17);
    nap<P_D5>();
    for (;;) {
      ldc4<0>(p0, ws, W_H5 + fo); ldc4<1024>(p1, ws, W_H5 + fo); ldc4<2048>(p2, ws, W_H5 + fo); ldc4<3072>(p3, ws, W_H5 + fo);
      WAITV(0);
      PIN(p0); PIN(p1); PIN(p2); PIN(p3);
      if (none(umax(umax(umax4(p0), umax4(p1)), umax(umax4(p2), umax4(p3))) == SENT) || ++spins > SPIN_MAX) break;
    }
    const f32x4 s = __builtin_bit_cast(f32x4, p0) + __builtin_bit_cast(f32x4, p1) + __builtin_bit_cast(f32x4, p2) + __builtin_bit_cast(f32x4, p3);
    const float o = lo(xo) + rbf(rbf(red4(s)) * gate);
    if (!(lane & 8)) *(u16*)(a.out + (fb * 512 + fm) * 2) = (u16)pk(o, o);
    if (h == 24 && j == 0 && lane == 0) stc1(ws, 0, tag);
  }
  STAMP(18);
}

extern "C" void kernel_launch(void* const* d_in, const int* in_sizes, int n_in, void* d_out, int out_size, void* d_ws, size_t ws_size, hipStream_t stream) {
  Args a;
  a.x = (const char*)d_in[0];
  a.ckv = (const char*)d_in[1];
  a.wq = (const char*)d_in[2];
  a.wuk = (const char*)d_in[3];
  a.wuv = (const char*)d_in[4];
  a.wo = (const char*)d_in[5];
  a.w1 = (const char*)d_in[7];
  a.b1 = (const char*)d_in[8];
  a.w2 = (const char*)d_in[9];
  a.b2 = (const char*)d_in[10];
  a.out = (char*)d_out;
  a.ws = (char*)d_ws;
  hipFuncSetAttribute((const void*)mla_ffn_decode, hipFuncAttributeMaxDynamicSharedMemorySize, L_TOTAL);
  mla_ffn_decode<<<256, 512, L_TOTAL, stream>>>(a);
}
```

```cpp
#include <hip/hip_runtime.h>
#include <hip/hip_bf16.h>
#include <stdint.h>

typedef unsigned u32;
typedef unsigned short u16;
typedef u32 u32x2 __attribute__((ext_vector_type(2)));
typedef u32 u32x4 __attribute__((ext_vector_type(4)));
typedef float f32x2 __attribute__((ext_vector_type(2)));
typedef float f32x4 __attribute__((ext_vector_type(4)));
typedef __bf16 bf16x2 __attribute__((ext_vector_type(2)));
typedef __bf16 bf16x8 __attribute__((ext_vector_type(8)));
typedef short s16x4 __attribute__((ext_vector_type(4)));
#define LAS __attribute__((address_space(3)))
#define DI __device__ __forceinline__

#define P_W 6
#define P_ST 1
#define P_OFF 0
#define P_DS 0
#define KVNT 0
#define P_D1 12
#define P_D2 14
#define P_D3 8
#define P_D4 12
#define P_D5 40
#define P_WO 1
#define P_UKL 1
#define P_D3B 0
constexpr u32 SPIN_MAX = 1u << 16;
constexpr u32 SENT = 0xFFFFFFFFu;

constexpr u32 W_H1 = 0x1000, W_H2 = W_H1 + 0x80000, W_ST = W_H2 + 0x40000, W_HF = W_ST + 0x10000;
constexpr u32 W_H4 = W_HF + 0x40000, W_H5 = W_H4 + 0x40000, W_HY = W_H5 + 0x40000, W_H3 = W_HY + 0x40000;
constexpr u32 W_DG = W_H3 + 0x400000;

constexpr u32 L_X = 131072, L_Y = L_X + 16384, L_TOTAL = L_Y + 16384;
constexpr u32 L_Q0 = 31 * 1024, L_XN = 63 * 1024, L_FL = 95 * 1024;
constexpr u32 L_W1 = L_X, L_MX = L_X + 8192, L_LS = L_X + 8704, L_P = L_X + 9216, L_CB = L_X + 13312, L_CX = L_X + 14336, L_HB = L_X + 15360;
constexpr u32 L_WO = 0, L_W2 = 16384;

struct Args {
  const char *x, *ckv, *wq, *wuk, *wuv, *wo, *w1, *b1, *w2, *b2;
  char *out, *ws;
};

#define WAITV(n) asm volatile("s_waitcnt vmcnt(%0)" ::"i"(n) : "memory")
#define BARRIER() asm volatile("s_waitcnt lgkmcnt(0)\n\ts_barrier" ::: "memory")
#define PIN(v) asm volatile("" : "+v"(v))
#define FENCE() __builtin_amdgcn_sched_barrier(0)

template <int NT> DI void dma(const char* base, u32 voff, u32 lds) {
  u32 keep;
  lds = __builtin_amdgcn_readfirstlane(lds);
  if (NT)
    asm volatile("s_mov_b32 %0, m0\n\ts_mov_b32 m0, %3\n\ts_nop 0\n\tglobal_load_lds_dwordx4 %1, %2 nt\n\ts_mov_b32 m0, %0"
                 : "=&s"(keep) : "v"(voff), "s"(base), "s"(lds) : "memory");
  else
    asm volatile("s_mov_b32 %0, m0\n\ts_mov_b32 m0, %3\n\ts_nop 0\n\tglobal_load_lds_dwordx4 %1, %2\n\ts_mov_b32 m0, %0"
                 : "=&s"(keep) : "v"(voff), "s"(base), "s"(lds) : "memory");
}
template <int OFF> DI void ldc4(u32x4& d, const char* base, u32 voff) {
  asm volatile("global_load_dwordx4 %0, %1, %2 offset:%3 sc1" : "=&v"(d) : "v"(voff), "s"(base), "i"(OFF) : "memory");
}
template <int OFF> DI void ldc2(u32x2& d, const char* base, u32 voff) {
  asm volatile("global_load_dwordx2 %0, %1, %2 offset:%3 sc1" : "=&v"(d) : "v"(voff), "s"(base), "i"(OFF) : "memory");
}
DI void ldg4(u32x4& d, const char* base, u32 voff) {
  asm volatile("global_load_dwordx4 %0, %1, %2" : "=&v"(d) : "v"(voff), "s"(base) : "memory");
}
DI void ldg2(u32x2& d, const char* base, u32 voff) {
  asm volatile("global_load_dwordx2 %0, %1, %2" : "=&v"(d) : "v"(voff), "s"(base) : "memory");
}
DI void ldgh(u32& d, const char* base, u32 voff) {
  asm volatile("global_load_ushort %0, %1, %2" : "=&v"(d) : "v"(voff), "s"(base) : "memory");
}
DI void stc4(const char* base, u32 voff, u32x4 v) {
  asm volatile("global_store_dwordx4 %0, %1, %2 sc1\n\ts_nop 1" ::"v"(voff), "v"(v), "s"(base) : "memory");
}
DI void stc2(const char* base, u32 voff, u32x2 v) {
  asm volatile("global_store_dwordx2 %0, %1, %2 sc1" ::"v"(voff), "v"(v), "s"(base) : "memory");
}
DI void stc1(const char* base, u32 voff, u32 v) {
  asm volatile("global_store_dword %0, %1, %2 sc1" ::"v"(voff), "v"(v), "s"(base) : "memory");
}

DI u32x4 lr4(u32 a) { return *(LAS u32x4*)a; }
DI void lw4(u32 a, u32x4 v) { *(LAS u32x4*)a = v; }
DI void lw2(u32 a, u32x2 v) { *(LAS u32x2*)a = v; }
DI void lw1(u32 a, u32 v) { *(LAS u32*)a = v; }
DI u32x4 ltr(u32 a0, u32 a1) {
  u32x2 p = __builtin_bit_cast(u32x2, __builtin_amdgcn_ds_read_tr16_b64_v4i16((LAS s16x4*)a0));
  u32x2 q = __builtin_bit_cast(u32x2, __builtin_amdgcn_ds_read_tr16_b64_v4i16((LAS s16x4*)a1));
  return u32x4{p.x, p.y, q.x, q.y};
}
DI f32x4 mfma(u32x4 a, u32x4 b, f32x4 c) {
  return __builtin_amdgcn_mfma_f32_16x16x32_bf16(__builtin_bit_cast(bf16x8, a), __builtin_bit_cast(bf16x8, b), c, 0, 0, 0);
}
DI u32 fu(float f) { return __float_as_uint(f); }
DI float uf(u32 u) { return __uint_as_float(u); }
DI u32 pk(float a, float b) { return __builtin_bit_cast(u32, __builtin_convertvector(f32x2{a, b}, bf16x2)); }
DI float lo(u32 u) { return uf(u << 16); }
DI float hi(u32 u) { return uf(u & 0xffff0000u); }
DI float rbf(float a) { return lo(pk(a, a)); }
DI u32 canon(u32 u) { return u < SENT - 1 ? u : SENT - 1; }
DI u32x4 canon4(f32x4 v) { return u32x4{canon(fu(v.x)), canon(fu(v.y)), canon(fu(v.z)), canon(fu(v.w))}; }
#pragma float_control(push)
#pragma float_control(precise, off)
DI float fmx(float a, float b) { return __builtin_fmaxf(a, b); }
#pragma float_control(pop)
DI u32 umax(u32 a, u32 b) { return a > b ? a : b; }
DI u32 umax4(u32x4 v) { return umax(umax(v.x, v.y), umax(v.z, v.w)); }
template <int C> DI float ror(float v) { return uf((u32)__builtin_amdgcn_update_dpp(0, (int)fu(v), 0x120 + C, 0xf, 0xf, true)); }
template <int BANKS> DI u32 ror8m(u32 keep, u32 src) { return (u32)__builtin_amdgcn_update_dpp((int)keep, (int)src, 0x128, 0xf, BANKS, false); }
DI float add32(float a, float b) { auto s = __builtin_amdgcn_permlane32_swap(fu(a), fu(b), false, false); return uf(s[0]) + uf(s[1]); }
DI float add16(float a, float b) { auto s = __builtin_amdgcn_permlane16_swap(fu(a), fu(b), false, false); return uf(s[0]) + uf(s[1]); }
DI float max32(float a) { auto s = __builtin_amdgcn_permlane32_swap(fu(a), fu(a), false, false); return fmx(uf(s[0]), uf(s[1])); }
DI float max16(float a) { auto s = __builtin_amdgcn_permlane16_swap(fu(a), fu(a), false, false); return fmx(uf(s[0]), uf(s[1])); }
DI float wsum(float v) {
  v += ror<8>(v); v += ror<4>(v); v += ror<2>(v); v += ror<1>(v);
  v = add16(v, v);
  return add32(v, v);
}
DI f32x4 sel(bool c, f32x4 a, f32x4 b) { return f32x4{c ? a.x : b.x, c ? a.y : b.y, c ? a.z : b.z, c ? a.w : b.w}; }
DI void lds_signal(u32 a) {
  asm volatile("s_waitcnt lgkmcnt(0)" ::: "memory");
  lw1(a, 1u);
}
DI void lds_wait4(u32 a, u32& spins) {
  for (;;) {
    asm volatile("" ::: "memory");
    const u32x4 f = lr4(a);
    if (__builtin_amdgcn_readfirstlane(f.x & f.y & f.z & f.w) || ++spins > SPIN_MAX) break;
  }
  asm volatile("" ::: "memory");
}
DI bool none(bool bad) { return __builtin_amdgcn_ballot_w64(bad) == 0; }
DI float ex2(float v) { return __builtin_amdgcn_exp2f(v); }
DI float rcp(float v) { return __builtin_amdgcn_rcpf(v); }
template <int N> DI void nap() { if (N > 0) __builtin_amdgcn_s_sleep(N); }

DI void red8(const float (&a)[8], float& o0, float& o1) {
  float r0 = add32(a[0], a[4]), r1 = add32(a[1], a[5]), r2 = add32(a[2], a[6]), r3 = add32(a[3], a[7]);
  float s0 = add16(r0, r2), s1 = add16(r1, r3);
  o0 = s0 + ror<8>(s0);
  o1 = s1 + ror<8>(s1);
}
DI float red4(f32x4 a) {
  float r0 = add32(a.x, a.z), r1 = add32(a.y, a.w);
  float s = add16(r0, r1);
  return s + ror<8>(s);
}

#define STAMP(i)
#define STAMPW(i)

__global__ __launch_bounds__(512, 2) void mla_ffn_decode(Args a) {
  const u32 tid = threadIdx.x, bid = blockIdx.x, lane = tid & 63;
  const u32 w = __builtin_amdgcn_readfirstlane(tid >> 6);
  STAMP(0);
  const u32 xcd = bid & 7, r = bid >> 3, grp = r & 1, j = (r >> 1) & 7, h = grp * 16 + xcd * 2 + (r >> 4);
  const u32 ab = xcd >> 1, aks = (xcd & 1) * 16 + (r >> 1), hg = 1 - grp, sp = ab * 32 + aks;
  const u32 mq = j >> 1, dh = j & 1;
  const u32 l15 = lane & 15, g = lane >> 4, bq = lane & 3, u = (lane >> 2) & 3, l7 = lane & 7, l3 = lane >> 3;
  const u32 tq = u, tp = bq;
  const bool ffn = w == 3 && h < 28, fin = w == 7 && h >= 24;
  if (w == 0) lw2(L_FL + lane * 8, u32x2{0, 0});
  BARRIER();
  const char* ws = a.ws;
  const u32 tag = *(const __attribute__((address_space(4))) u32*)ws + 1;
  u32 spins = 0;

  const u32 f1 = (l3 >> 1) & 1;
  const u32 trn = (g * 8 + tq) * 128 + (tp & 1) * 8, trf = ((tq >> 1) | ((g & 1) << 1)) << 1;
  const u32x4 sent4 = {SENT, SENT, SENT, SENT};
  u32 o5, ohf, ost, o3, o3b, bfr, va, frag[4];
  u32x4 xf0, xf1;
#define COMMON_ADDR()                                                                                        \
  do {                                                                                                       \
    o5 = (j * 8 + bq * 2 + (u >> 1)) * 4096 + h * 128 + (u & 1) * 64 + g * 16;                               \
    ohf = W_HF + h * 8192 + j * 1024 + lane * 16;                                                            \
    ost = W_ST + (hg * 16 + l15) * 2048 + sp * 16;                                                           \
    o3 = W_H3 + (((hg * 16 + l7) * 8 + w) * 128 + sp) * 128 + (((lane >> 3) & 1) * 4 + g) * 16;              \
    o3b = o3 + 8 * 8 * 16384;                                                                                \
    bfr = g * 64 + bq * 16;                                                                                  \
    va = (g * 4 + tq) * 1024 + (((w * 8 + (tp >> 1)) ^ (((g & 1) * 4 + tq) << 1)) << 4) + (tp & 1) * 8;      \
    _Pragma("unroll") for (int i = 0; i < 4; ++i) frag[i] = (l15 * 1024 + ((g ^ (l7 << 1)) << 4)) ^ (i << 6); \
    PIN(o5); PIN(ohf); PIN(ost); PIN(o3); PIN(o3b); PIN(bfr); PIN(va);                                       \
    PIN(frag[0]); PIN(frag[1]); PIN(frag[2]); PIN(frag[3]);                                                  \
  } while (0)
  const char* kvb = a.ckv + (size_t)(ab * 4096 + aks * 128 + (w & 3) * 32) * 1024;

  if (w < 4) {
    u32x4 xv;
    ldg4(xv, a.x, w * 1024 + lane * 16);
    const u32 vq = ((mq * 128 + w * 32 + l3) * 32 + h) * 256 + dh * 128;
    const u32 vq0 = vq + ((l7 ^ (f1 << 1)) << 4), vq1 = vq + 8 * 8192 + ((l7 ^ ((f1 | 2) << 1)) << 4);
    dma<1>(a.wq, vq0, L_X + w * 4096);
    dma<1>(a.wq, vq1, L_X + w * 4096 + 1024);
    dma<1>(a.wq + 16 * 8192, vq0, L_X + w * 4096 + 2048);
    dma<1>(a.wq + 16 * 8192, vq1, L_X + w * 4096 + 3072);
#define UK_DMA()                                                                                              \
  _Pragma("unroll") for (int i = 0; i < 4; ++i)                                                               \
      dma<1>(a.wuk + i * 4 * 8192, ((j * 64 + w * 16 + g) * 32 + h) * 256 + ((l15 ^ (i * 4 + g)) << 4), L_Y + w * 4096 + i * 1024)
    if (!P_UKL) { UK_DMA(); }
    COMMON_ADDR();
    u32 oc = w == 0 ? ohf : w == 1 ? W_H4 + h * 8192 + l3 * 1024 + j * 128 + l7 * 16 : w == 2 ? W_H5 + o5 : W_HY + o5;
    u32 taq = L_X + trn + (((2 * w + (tp >> 1)) ^ trf) << 4);
    u32 o1s = W_H1 + h * 16384 + dh * 8192 + w * 2048 + mq * 512 + (g * 8 + l7) * 16;
    u32 o1 = W_H1 + h * 16384 + w * 4096 + lane * 16;
    u32 iq = L_Q0 + (4 * w + (lane >> 5) * 2 + (g & 1)) * 64 + bq * 16 + (l3 & 1) * 8 + (u & 1) * 4;
    u32 ua = L_Y + (w * 32 + l15) * 256 + ((g ^ l15) << 4);
    u32 o2s = W_H2 + (bq * 32 + h) * 2048 + j * 256 + w * 128 + u * 64 + g * 16;
    u32 o2 = W_H2 + (ab * 32 + hg * 16 + 4 * w) * 2048 + lane * 16, o2b = o2 + 4096;
    u32 i0 = L_X + 4 * w * 1024 + (((lane >> 1) ^ (((4 * w) & 7) << 1)) << 4) + (lane & 1) * 8;
    PIN(oc); PIN(taq); PIN(o1s); PIN(o1); PIN(iq); PIN(ua); PIN(o2s); PIN(o2); PIN(o2b); PIN(i0);

    WAITV(P_UKL ? 4 : 8);
    STAMP(19);
    PIN(xv);
    {
      float v[8] = {lo(xv.x), hi(xv.x), lo(xv.y), hi(xv.y), lo(xv.z), hi(xv.z), lo(xv.w), hi(xv.w)};
      const float mean = wsum(((v[0] + v[1]) + (v[2] + v[3])) + ((v[4] + v[5]) + (v[6] + v[7]))) * (1.f / 512.f);
      float d[8];
#pragma unroll
      for (int i = 0; i < 8; ++i) { d[i] = v[i] - mean; d[i] *= d[i]; }
      const float var = rbf(wsum(((d[0] + d[1]) + (d[2] + d[3])) + ((d[4] + d[5]) + (d[6] + d[7]))) * (1.f / 512.f)), mb = rbf(mean);
      const float inv = rcp(rbf(__builtin_amdgcn_sqrtf(rbf(var + 1e-5f))));
      u32x4 o;
#pragma unroll
      for (int i = 0; i < 4; ++i) { const u32 c = pk(v[2 * i] - mb, v[2 * i + 1] - mb); o[i] = pk(lo(c) * inv, hi(c) * inv); }
      PIN(o);
      if (g == mq) lw4(L_XN + l15 * 64 + w * 16, o);
    }
    WAITV(P_UKL ? 0 : 4);
    STAMP(1);
    lds_signal(L_FL + w * 4);
    if (P_UKL == 1) { UK_DMA(); }
    lds_wait4(L_FL, spins);
    {
      u32x4 fa[4], fb[4];
#pragma unroll
      for (int ks = 0; ks < 4; ++ks) { fa[ks] = ltr(taq + ks * 4096, taq + ks * 4096 + 512); fb[ks] = lr4(L_XN + ks * 256 + bfr); }
      FENCE();
      f32x4 c0 = mfma(fa[0], fb[0], f32x4{0, 0, 0, 0}), c1 = mfma(fa[1], fb[1], f32x4{0, 0, 0, 0});
      c0 = mfma(fa[2], fb[2], c0);
      c1 = mfma(fa[3], fb[3], c1);
      xf0 = dh ? fb[2] : fb[0];
      xf1 = dh ? fb[3] : fb[1];
      const f32x4 acc = c0 + c1;
      u32x4 o = {fu((u & 1) ? acc.z : acc.x), tag, fu((u & 1) ? acc.w : acc.y), tag};
      PIN(o);
      if (u < 2) stc4(ws, o1s, o);
    }
    STAMP(2);
    if (P_ST == 1) lw1(L_FL + 32 + w * 4, 1u);
    if (P_UKL == 2) { UK_DMA(); }
    stc4(ws, oc, sent4);
    {
      u32x4 p0, p1, p2, p3;
      nap<P_D1>();
      for (;;) {
        ldc4<0>(p0, ws, o1); ldc4<1024>(p1, ws, o1); ldc4<2048>(p2, ws, o1); ldc4<3072>(p3, ws, o1);
        if (P_ST == 2) lw1(L_FL + 32 + w * 4, 1u);
        WAITV(0);
        PIN(p0); PIN(p1); PIN(p2); PIN(p3);
        const u32 bad = (p0.y ^ tag) | (p0.w ^ tag) | (p1.y ^ tag) | (p1.w ^ tag) | (p2.y ^ tag) | (p2.w ^ tag) | (p3.y ^ tag) | (p3.w ^ tag);
        if (none(bad != 0) || ++spins > SPIN_MAX) break;
      }
      lw1(iq, pk(add32(uf(p0.x) + uf(p1.x), uf(p2.x) + uf(p3.x)), add32(uf(p0.z) + uf(p1.z), uf(p2.z) + uf(p3.z))));
    }
    STAMP(3);
    if (P_ST == 3) lw1(L_FL + 32 + w * 4, 1u);
    lds_signal(L_FL + 16 + w * 4);
    if (w < 2) {
      u32x4 fb[4], f0[4], f1v[4];
      lds_wait4(L_FL + 16, spins);
#pragma unroll
      for (int ks = 0; ks < 4; ++ks) { fb[ks] = lr4(L_Q0 + bfr + ks * 256); f0[ks] = lr4(ua ^ (ks << 6)); f1v[ks] = lr4((ua ^ (ks << 6)) + 4096); }
      FENCE();
      f32x4 c0 = {0, 0, 0, 0}, c1 = {0, 0, 0, 0};
#pragma unroll
      for (int ks = 0; ks < 4; ++ks) { c0 = mfma(f0[ks], fb[ks], c0); c1 = mfma(f1v[ks], fb[ks], c1); }
      const float sc = 0.08838834764831845f * 1.4426950408889634f;
      const f32x4 c = sel(u & 1, c1, c0) * sc;
      u32x4 o = {pk(c.x, c.y), tag, pk(c.z, c.w), tag};
      PIN(o);
      if (u < 2) stc4(ws, o2s, o);
      STAMP(4);
      nap<P_D2>();
    } else {
      lds_wait4(L_FL + 16, spins);
      nap<P_D2 + 6>();
    }
    u32x4 s[8];
    for (;;) {
      ldc4<0>(s[0], ws, o2); ldc4<1024>(s[1], ws, o2); ldc4<2048>(s[2], ws, o2); ldc4<3072>(s[3], ws, o2);
      ldc4<0>(s[4], ws, o2b); ldc4<1024>(s[5], ws, o2b); ldc4<2048>(s[6], ws, o2b); ldc4<3072>(s[7], ws, o2b);
      WAITV(0);
      u32 bad = 0;
#pragma unroll
      for (int i = 0; i < 8; ++i) { PIN(s[i]); bad |= (s[i].y ^ tag) | (s[i].w ^ tag); }
      if (none(bad != 0) || ++spins > SPIN_MAX) break;
    }
#pragma unroll
    for (int i = 0; i < 8; ++i) lw2((i0 ^ ((i >> 1) << 5)) + (i >> 1) * 1024 + (i & 1) * 512, u32x2{s[i].x, s[i].z});
    STAMP(5);
    BARRIER();
  } else {
    COMMON_ADDR();
    if (P_ST) lds_wait4(L_FL + 32, spins);
    nap<P_DS>();
    STAMP(2);
    const u32 lb = (w & 3) * 32768, off = grp ? P_OFF : 0;
#pragma unroll
    for (int i = 0; i < 31; ++i) {
      if (i >= P_W) WAITV(P_W - 1);
      const u32 row = i < 31 - P_OFF ? i + off : i - (31 - P_OFF) + (grp ? 0 : 31 - P_OFF);
      dma<KVNT>(kvb + row * 1024, (lane ^ ((row & 7) << 1)) << 4, lb + row * 1024);
    }
    STAMP(4);
    BARRIER();
    dma<KVNT>(kvb + 31 * 1024, (lane ^ 14) << 4, lb + 31 * 1024);
  }
  stc4(ws, o3, sent4);
  stc4(ws, o3b, sent4);
  const u32 lkv = w * 16384;
  const u32 vuk = ((j * 64 + w * 8 + g) * 32 + h) * 256;
  dma<1>(a.wuv, vuk + ((l15 ^ (g << 1)) << 4), L_Y + w * 2048);
  dma<1>(a.wuv, vuk + 4 * 8192 + ((l15 ^ ((4 + g) << 1)) << 4), L_Y + w * 2048 + 1024);

  u32x4 fr[16];
#pragma unroll
  for (int ks = 0; ks < 16; ++ks) fr[ks] = lr4(L_X + frag[ks & 3] + (ks >> 2) * 256);
#pragma unroll
  for (int i = 0; i < 4; ++i) { frag[i] += lkv; PIN(frag[i]); }
  STAMP(6);
  BARRIER();
  if (ffn) {
#pragma unroll
    for (int i = 0; i < 8; ++i)
      dma<1>(a.w1 + i * 8 * 3584, (j * 64 + l3) * 3584 + h * 128 + ((l7 ^ ((f1 | ((i & 1) << 1)) << 1)) << 4), L_W1 + i * 1024);
  }
  u32 imx = L_MX + l15 * 32, ip = L_P + lane * 16;
  PIN(imx); PIN(ip);
  if (w >= 4) WAITV(4);
  STAMP(7);
  BARRIER();

  f32x4 sv;
  {
    u32x4 fa[16];
#pragma unroll
    for (int ks = 0; ks < 16; ++ks) fa[ks] = lr4(frag[ks & 3] + (ks >> 2) * 256);
    FENCE();
    f32x4 c0 = {0, 0, 0, 0}, c1 = {0, 0, 0, 0};
#pragma unroll
    for (int ks = 0; ks < 16; ks += 2) { c0 = mfma(fa[ks], fr[ks], c0); c1 = mfma(fa[ks + 1], fr[ks + 1], c1); }
    sv = c0 + c1;
    float m = max32(max16(fmx(fmx(sv.x, sv.y), fmx(sv.z, sv.w))));
    PIN(m);
    if (g == 0) lw1(imx + w * 4, fu(m));
  }
  STAMP(8);
  BARRIER();
  float mx;
  {
    const u32x4 m0 = lr4(imx), m1 = lr4(imx + 16);
    FENCE();
#pragma unroll
    for (int ks = 0; ks < 4; ++ks)
#pragma unroll
      for (int t = 0; t < 4; ++t) fr[t * 4 + ks] = ltr((va ^ (t << 5)) + ks * 32768, (va ^ (t << 5)) + ks * 32768 + 16384);
    FENCE();
    mx = fmx(fmx(fmx(uf(m0.x), uf(m0.y)), fmx(uf(m0.z), uf(m0.w))), fmx(fmx(uf(m1.x), uf(m1.y)), fmx(uf(m1.z), uf(m1.w))));
    const u32 p0 = pk(ex2(sv.x - mx), ex2(sv.y - mx)), p1 = pk(ex2(sv.z - mx), ex2(sv.w - mx));
    lw2(ip + (w >> 1) * 1024 + (w & 1) * 8, u32x2{p0, p1});
    float l = (lo(p0) + hi(p0)) + (lo(p1) + hi(p1));
    l = add16(l, l);
    l = add32(l, l);
    PIN(l);
    if (g == 0) lw1(imx + (L_LS - L_MX) + w * 4, fu(l));
    if (ffn) WAITV(10); else WAITV(2);
  }
  STAMP(9);
  BARRIER();
#define WO_DMA()                                                                          \
  do {                                                                                    \
    if (ffn) {                                                                            \
      _Pragma("unroll") for (int i = 0; i < 8; ++i)                                       \
          dma<1>(a.w2 + i * 8192, (h * 64 + l3) * 1024 + j * 128 + ((l7 ^ (l3 & 6)) << 4), L_W2 + i * 1024); \
    }                                                                                     \
    const u32 vo = (h * 128 + w * 16 + l3) * 1024 + j * 128;                              \
    dma<1>(a.wo, vo + ((l7 ^ (f1 << 1)) << 4), L_WO + w * 2048);                          \
    dma<1>(a.wo, vo + 8 * 1024 + ((l7 ^ ((f1 | 2) << 1)) << 4), L_WO + w * 2048 + 1024);  \
  } while (0)
  {
    u32x4 pf[4];
#pragma unroll
    for (int ks = 0; ks < 4; ++ks) pf[ks] = lr4(ip + ks * 1024);
    if (w == 7) {
      const u32x4 l0 = lr4(imx + (L_LS - L_MX)), l1 = lr4(imx + (L_LS - L_MX) + 16);
      u32x4 o = {fu(mx), tag, fu(((uf(l0.x) + uf(l0.y)) + (uf(l0.z) + uf(l0.w))) + ((uf(l1.x) + uf(l1.y)) + (uf(l1.z) + uf(l1.w)))), tag};
      PIN(o);
      if (lane < 16) stc4(ws, ost, o);
    }
    FENCE();
    f32x4 o[4] = {{0, 0, 0, 0}, {0, 0, 0, 0}, {0, 0, 0, 0}, {0, 0, 0, 0}};
#pragma unroll
    for (int ks = 0; ks < 4; ++ks)
#pragma unroll
      for (int t = 0; t < 4; ++t) o[t] = mfma(fr[t * 4 + ks], pf[ks], o[t]);
    const u32x4 r0 = {pk(o[0].x, o[0].y), pk(o[0].z, o[0].w), pk(o[1].x, o[1].y), pk(o[1].z, o[1].w)};
    const u32x4 r1 = {pk(o[2].x, o[2].y), pk(o[2].z, o[2].w), pk(o[3].x, o[3].y), pk(o[3].z, o[3].w)};
    u32x4 n0, n1;
#pragma unroll
    for (int e = 0; e < 4; ++e) { n0[e] = canon(ror8m<0xC>(r0[e], r1[e])); n1[e] = canon(ror8m<0x3>(r1[e], r0[e])); }
    stc4(ws, o3, n0);
    stc4(ws, o3b, n1);
  }
  STAMP(10);
  if (P_WO == 0) WO_DMA();

  const u32 fb = (h >> 1) & 3, fm = j * 64 + (h & 1) * 32 + l7 * 4 + (lane >> 5) * 2 + ((lane >> 4) & 1);
  u32 osn = W_ST + h * 2048 + ((w >> 1) * 32 + l3) * 16 + (w & 1) * 256, oso = osn ^ 256;
  u32 od = W_H3 + ((h * 8 + j) * 128 + w * 16) * 128 + lane * 16;
  u32 icb = L_CB + l7 * 128 + ((((w & 1) * 4 + (w >> 1)) ^ l7) << 4) + (lane >> 5) * 8 + ((lane >> 4) & 1) * 4;
  u32 vu = L_Y + (g * 4 + tq) * 256 + (((2 * w + (tp >> 1)) ^ (((g & 1) * 4 + tq) << 1)) << 4) + (tp & 1) * 8;
  u32 rcb = L_CB + g * 128 + ((l7 ^ g) << 4);
  u32 o4s = W_H4 + h * 8192 + w * 1024 + j * 128 + bq * 32 + g * 8;
  PIN(osn); PIN(oso); PIN(od); PIN(icb); PIN(vu); PIN(rcb); PIN(o4s);
  if (ffn) {
    WAITV(P_WO == 0 ? 12 : 2);
    u32x4 fa[8];
#pragma unroll
    for (int t = 0; t < 4; ++t) {
      const u32 ta = L_W1 + trn + (((2 * t + (tp >> 1)) ^ trf) << 4);
      fa[2 * t] = ltr(ta, ta + 512);
      fa[2 * t + 1] = ltr(ta + 4096, ta + 4096 + 512);
    }
    FENCE();
    f32x4 d[4];
#pragma unroll
    for (int t = 0; t < 4; ++t) d[t] = mfma(fa[2 * t], xf0, f32x4{0, 0, 0, 0});
#pragma unroll
    for (int t = 0; t < 4; ++t) d[t] = mfma(fa[2 * t + 1], xf1, d[t]);
    stc4(ws, ohf, canon4(sel(u & 2, sel(u & 1, d[3], d[2]), sel(u & 1, d[1], d[0]))));
    nap<(P_D3 > 10 ? P_D3 - 10 : 0)>();
  } else {
    nap<P_D3>();
  }
  {
    u32x4 e0, e1, e2, e3, d0, d1;
    STAMP(11);
    for (;;) {
      ldc4<0>(e0, ws, osn); ldc4<128>(e1, ws, osn); ldc4<0>(e2, ws, oso); ldc4<128>(e3, ws, oso);
      WAITV(0);
      PIN(e0); PIN(e1); PIN(e2); PIN(e3);
      const u32 bad = (e0.y ^ tag) | (e0.w ^ tag) | (e1.y ^ tag) | (e1.w ^ tag) | (e2.y ^ tag) | (e2.w ^ tag) | (e3.y ^ tag) | (e3.w ^ tag);
      if (none(bad != 0) || ++spins > SPIN_MAX) break;
    }
    STAMP(17);
    nap<P_D3B>();
    ldc4<0>(d0, ws, od); ldc4<1024>(d1, ws, od);
    if (P_WO == 1) WO_DMA();
    float m = fmx(fmx(uf(e0.x), uf(e1.x)), fmx(uf(e2.x), uf(e3.x)));
    m = max32(max16(fmx(m, ror<8>(m))));
    float w0 = ex2(uf(e0.x) - m), w1 = ex2(uf(e1.x) - m);
    float l = w0 * uf(e0.z) + w1 * uf(e1.z) + ex2(uf(e2.x) - m) * uf(e2.z) + ex2(uf(e3.x) - m) * uf(e3.z);
    l += ror<8>(l);
    l = add16(l, l);
    l = add32(l, l);
    const float il = rcp(l);
    w0 *= il; w1 *= il;
    PIN(w0); PIN(w1);
    if (P_WO != 1) WAITV(0); else if (ffn) WAITV(10); else WAITV(2);
    for (;;) {
      PIN(d0); PIN(d1);
      if (none(umax(umax4(d0), umax4(d1)) == SENT) || ++spins > SPIN_MAX) break;
      ldc4<0>(d0, ws, od); ldc4<1024>(d1, ws, od);
      WAITV(0);
    }
    STAMP(12);
    if (P_WO == 2) WO_DMA();
    float acc[8];
#pragma unroll
    for (int e = 0; e < 4; ++e) {
      acc[2 * e] = w0 * lo(d0[e]) + w1 * lo(d1[e]);
      acc[2 * e + 1] = w0 * hi(d0[e]) + w1 * hi(d1[e]);
    }
    float c0, c1;
    red8(acc, c0, c1);
    u32 o = pk(c0, c1);
    PIN(o);
    if (!(lane & 8)) lw1(icb, o);
  }
  STAMP(13);
  BARRIER();
  {
    const u32x4 a0 = ltr(vu, vu + 4096), b0 = lr4(rcb), a1 = ltr(vu + 8192, vu + 8192 + 4096), b1 = lr4((rcb ^ 64) + 512);
    FENCE();
    const f32x4 c = mfma(a0, b0, f32x4{0, 0, 0, 0}) + mfma(a1, b1, f32x4{0, 0, 0, 0});
    u32x2 v = {canon(pk(c.x + ror<4>(c.x), c.y + ror<4>(c.y))), canon(pk(c.z + ror<4>(c.z), c.w + ror<4>(c.w)))};
    PIN(v);
    if (u == 0) stc2(ws, o4s, v);
  }
  STAMP(14);
  if (P_WO != 0) { WAITV(1); BARRIER(); }
  if (w < 2) {
    const u32 t0 = L_WO + trn + (((4 * w + (tp >> 1)) ^ trf) << 4), t1 = t0 ^ 32;
    u32 o4 = W_H4 + h * 8192 + (g >> 1) * 1024 + u * 128 + bq * 32 + (g & 1) * 16, o4b = o4 + 4096;
    u32 o5s = W_H5 + (j * 8 + bq * 2 + w) * 4096 + h * 128 + u * 64 + g * 16;
    u32x4 fa[8];
#pragma unroll
    for (int ks = 0; ks < 4; ++ks) { fa[ks] = ltr(t0 + ks * 4096, t0 + ks * 4096 + 512); fa[4 + ks] = ltr(t1 + ks * 4096, t1 + ks * 4096 + 512); }
#pragma unroll
    for (int i = 0; i < 8; ++i) PIN(fa[i]);
    PIN(o4); PIN(o4b); PIN(o5s);
    nap<P_D4>();
    u32x4 p[8];
    for (;;) {
      ldc4<0>(p[0], ws, o4); ldc4<512>(p[1], ws, o4); ldc4<2048>(p[2], ws, o4); ldc4<2560>(p[3], ws, o4);
      ldc4<0>(p[4], ws, o4b); ldc4<512>(p[5], ws, o4b); ldc4<2048>(p[6], ws, o4b); ldc4<2560>(p[7], ws, o4b);
      WAITV(0);
      u32 t = 0;
#pragma unroll
      for (int i = 0; i < 8; ++i) { PIN(p[i]); t = umax(t, umax4(p[i])); }
      if (none(t == SENT) || ++spins > SPIN_MAX) break;
    }
    STAMP(15);
    f32x4 c[4] = {{0, 0, 0, 0}, {0, 0, 0, 0}, {0, 0, 0, 0}, {0, 0, 0, 0}};
#pragma unroll
    for (int ks = 0; ks < 4; ++ks) {
      c[0] = mfma(fa[ks], p[2 * ks], c[0]);
      c[1] = mfma(fa[4 + ks], p[2 * ks], c[1]);
      c[2] = mfma(fa[ks], p[2 * ks + 1], c[2]);
      c[3] = mfma(fa[4 + ks], p[2 * ks + 1], c[3]);
    }
    f32x4 c0 = c[0] + c[2], c1 = c[1] + c[3];
#pragma unroll
    for (int e = 0; e < 4; ++e) {
      c0[e] += ror<8>(c0[e]); c1[e] += ror<8>(c1[e]);
      c0[e] += ror<4>(c0[e]); c1[e] += ror<4>(c1[e]);
    }
    u32x4 o = canon4(sel(u & 1, c1, c0));
    PIN(o);
    if (u < 2) stc4(ws, o5s, o);
  } else if (ffn) {
    const u32 of = W_HF + h * 8192 + lane * 16;
    const u32 f2 = (((g & 1) << 1) | (tq >> 1)) << 1;
    u32x4 p[8], fa[8];
    u32x2 b1v;
    ldg2(b1v, a.b1, (h * 64 + u * 16 + g * 4) * 2);
    for (;;) {
      ldc4<0>(p[0], ws, of); ldc4<1024>(p[1], ws, of); ldc4<2048>(p[2], ws, of); ldc4<3072>(p[3], ws, of);
      ldc4<0>(p[4], ws, of + 4096); ldc4<1024>(p[5], ws, of + 4096); ldc4<2048>(p[6], ws, of + 4096); ldc4<3072>(p[7], ws, of + 4096);
#pragma unroll
      for (int t = 0; t < 4; ++t) {
        const u32 ta = L_W2 + (g * 4 + tq) * 128 + (((2 * t + (tp >> 1)) ^ f2) << 4) + (tp & 1) * 8;
        fa[2 * t] = ltr(ta, ta + 2048);
        fa[2 * t + 1] = ltr(ta + 4096, ta + 4096 + 2048);
      }
      WAITV(0);
      u32 t = 0;
#pragma unroll
      for (int i = 0; i < 8; ++i) { PIN(p[i]); t = umax(t, umax4(p[i])); }
      if (none(t == SENT) || ++spins > SPIN_MAX) break;
    }
    PIN(b1v);
    const float bb[4] = {lo(b1v.x), hi(b1v.x), lo(b1v.y), hi(b1v.y)};
    float ge[4];
#pragma unroll
    for (int e = 0; e < 4; ++e) {
      const float s = ((uf(p[0][e]) + uf(p[1][e])) + (uf(p[2][e]) + uf(p[3][e]))) + ((uf(p[4][e]) + uf(p[5][e])) + (uf(p[6][e]) + uf(p[7][e])));
      const float t = rbf(rbf(s) + bb[e]);
      const float z = ex2(2.302208198f * (t + 0.044715f * t * t * t));
      ge[e] = t - t * rcp(z + 1.f);
    }
    lw2(L_HB + (u >> 1) * 256 + bfr + (u & 1) * 8, u32x2{pk(ge[0], ge[1]), pk(ge[2], ge[3])});
    asm volatile("s_waitcnt lgkmcnt(0)" ::: "memory");
    const u32x4 hb0 = lr4(L_HB + bfr), hb1 = lr4(L_HB + 256 + bfr);
    f32x4 d[4];
#pragma unroll
    for (int t = 0; t < 4; ++t) d[t] = mfma(fa[2 * t], hb0, f32x4{0, 0, 0, 0});
#pragma unroll
    for (int t = 0; t < 4; ++t) d[t] = mfma(fa[2 * t + 1], hb1, d[t]);
    stc4(ws, W_HY + o5, canon4(sel(u & 2, sel(u & 1, d[3], d[2]), sel(u & 1, d[1], d[0]))));
    STAMP(15);
  } else if (fin) {
    u32 fy = W_HY + (j * 8 + (h & 7)) * 4096 + lane * 16, fy3 = lane < 32 ? fy + 3072 : fy, fs = fy + (W_H5 - W_HY);
    u32 oo = (fb * 512 + fm) * 2;
    u32 xo, b2o;
    ldgh(xo, a.x, oo);
    ldgh(b2o, a.b2, fm * 2);
    PIN(fy); PIN(fy3); PIN(fs); PIN(oo);
    nap<P_D5>();
    u32x4 p0, p1, p2, p3;
    for (;;) {
      ldc4<0>(p0, ws, fy); ldc4<1024>(p1, ws, fy); ldc4<2048>(p2, ws, fy); ldc4<0>(p3, ws, fy3);
      WAITV(0);
      PIN(p0); PIN(p1); PIN(p2); PIN(p3);
      if (none(umax(umax(umax4(p0), umax4(p1)), umax(umax4(p2), umax4(p3))) == SENT) || ++spins > SPIN_MAX) break;
    }
    STAMP(15);
    u32x4 q0, q1, q2, q3;
    ldc4<0>(q0, ws, fs); ldc4<1024>(q1, ws, fs); ldc4<2048>(q2, ws, fs); ldc4<3072>(q3, ws, fs);
    PIN(xo); PIN(b2o);
    const f32x4 z = {0, 0, 0, 0};
    const f32x4 sy = __builtin_bit_cast(f32x4, p0) + __builtin_bit_cast(f32x4, p1) + __builtin_bit_cast(f32x4, p2) + sel(lane < 32, __builtin_bit_cast(f32x4, p3), z);
    const float y = rbf(rbf(red4(sy)) + lo(b2o));
    float gate = rbf(y * rcp(1.f + ex2(-1.4426950408889634f * y)));
    PIN(gate);
    WAITV(0);
    for (;;) {
      PIN(q0); PIN(q1); PIN(q2); PIN(q3);
      if (none(umax(umax(umax4(q0), umax4(q1)), umax(umax4(q2), umax4(q3))) == SENT) || ++spins > SPIN_MAX) break;
      ldc4<0>(q0, ws, fs); ldc4<1024>(q1, ws, fs); ldc4<2048>(q2, ws, fs); ldc4<3072>(q3, ws, fs);
      WAITV(0);
    }
    const f32x4 s = (__builtin_bit_cast(f32x4, q0) + __builtin_bit_cast(f32x4, q1)) + (__builtin_bit_cast(f32x4, q2) + __builtin_bit_cast(f32x4, q3));
    const float o = lo(xo) + rbf(rbf(red4(s)) * gate);
    if (!(lane & 8)) *(u16*)(a.out + oo) = (u16)pk(o, o);
    if (h == 24 && j == 0 && lane == 0) stc1(ws, 0, tag);
  }
  STAMP(18);
}

extern "C" void kernel_launch(void* const* d_in, const int* in_sizes, int n_in, void* d_out, int out_size, void* d_ws, size_t ws_size, hipStream_t stream) {
  Args a;
  a.x = (const char*)d_in[0];
  a.ckv = (const char*)d_in[1];
  a.wq = (const char*)d_in[2];
  a.wuk = (const char*)d_in[3];
  a.wuv = (const char*)d_in[4];
  a.wo = (const char*)d_in[5];
  a.w1 = (const char*)d_in[7];
  a.b1 = (const char*)d_in[8];
  a.w2 = (const char*)d_in[9];
  a.b2 = (const char*)d_in[10];
  a.out = (char*)d_out;
  a.ws = (char*)d_ws;
  (void)hipFuncSetAttribute((const void*)mla_ffn_decode, hipFuncAttributeMaxDynamicSharedMemorySize, L_TOTAL);
  mla_ffn_decode<<<256, 512, L_TOTAL, stream>>>(a);
}
```

```cpp
#define AMP 2
#include <hip/hip_runtime.h>
#include <hip/hip_bf16.h>
#include <stdint.h>

typedef unsigned u32;
typedef unsigned short u16;
typedef u32 u32x2 __attribute__((ext_vector_type(2)));
typedef u32 u32x4 __attribute__((ext_vector_type(4)));
typedef float f32x2 __attribute__((ext_vector_type(2)));
typedef float f32x4 __attribute__((ext_vector_type(4)));
typedef __bf16 bf16x2 __attribute__((ext_vector_type(2)));
typedef __bf16 bf16x8 __attribute__((ext_vector_type(8)));
typedef short s16x4 __attribute__((ext_vector_type(4)));
#define LAS __attribute__((address_space(3)))
#define DI __device__ __forceinline__

#define P_W 6
#define P_ST 1
#define P_OFF 0
#define P_DS 0
#define KVNT 0
#define P_D1 12
#define P_D2 14
#define P_D3 8
#define P_D4 12
#define P_D5 40
#define P_WO 1
#define P_UKL 1
#define P_D3B 0
constexpr u32 SPIN_MAX = 1u << 16;
constexpr u32 SENT = 0xFFFFFFFFu;

constexpr u32 W_H1 = 0x1000, W_H2 = W_H1 + 0x80000, W_ST = W_H2 + 0x40000, W_HF = W_ST + 0x10000;
constexpr u32 W_H4 = W_HF + 0x40000, W_H5 = W_H4 + 0x40000, W_HY = W_H5 + 0x40000, W_H3 = W_HY + 0x40000;
constexpr u32 W_DG = W_H3 + 0x400000;

constexpr u32 L_X = 131072, L_Y = L_X + 16384, L_TOTAL = L_Y + 16384;
constexpr u32 L_Q0 = 31 * 1024, L_XN = 63 * 1024, L_FL = 95 * 1024;
constexpr u32 L_W1 = L_X, L_MX = L_X + 8192, L_LS = L_X + 8704, L_P = L_X + 9216, L_CB = L_X + 13312, L_CX = L_X + 14336, L_HB = L_X + 15360;
constexpr u32 L_WO = 0, L_W2 = 16384;

struct Args {
  const char *x, *ckv, *wq, *wuk, *wuv, *wo, *w1, *b1, *w2, *b2;
  char *out, *ws;
};

#define WAITV(n) asm volatile("s_waitcnt vmcnt(%0)" ::"i"(n) : "memory")
#define BARRIER() asm volatile("s_waitcnt lgkmcnt(0)\n\ts_barrier" ::: "memory")
#define PIN(v) asm volatile("" : "+v"(v))
#define FENCE() __builtin_amdgcn_sched_barrier(0)

template <int NT> DI void dma(const char* base, u32 voff, u32 lds) {
  u32 keep;
  lds = __builtin_amdgcn_readfirstlane(lds);
  if (NT)
    asm volatile("s_mov_b32 %0, m0\n\ts_mov_b32 m0, %3\n\ts_nop 0\n\tglobal_load_lds_dwordx4 %1, %2 nt\n\ts_mov_b32 m0, %0"
                 : "=&s"(keep) : "v"(voff), "s"(base), "s"(lds) : "memory");
  else
    asm volatile("s_mov_b32 %0, m0\n\ts_mov_b32 m0, %3\n\ts_nop 0\n\tglobal_load_lds_dwordx4 %1, %2\n\ts_mov_b32 m0, %0"
                 : "=&s"(keep) : "v"(voff), "s"(base), "s"(lds) : "memory");
}
template <int OFF> DI void ldc4(u32x4& d, const char* base, u32 voff) {
  asm volatile("global_load_dwordx4 %0, %1, %2 offset:%3 sc1" : "=&v"(d) : "v"(voff), "s"(base), "i"(OFF) : "memory");
}
template <int OFF> DI void ldc2(u32x2& d, const char* base, u32 voff) {
  asm volatile("global_load_dwordx2 %0, %1, %2 offset:%3 sc1" : "=&v"(d) : "v"(voff), "s"(base), "i"(OFF) : "memory");
}
DI void ldg4(u32x4& d, const char* base, u32 voff) {
  asm volatile("global_load_dwordx4 %0, %1, %2" : "=&v"(d) : "v"(voff), "s"(base) : "memory");
}
DI void ldg2(u32x2& d, const char* base, u32 voff) {
  asm volatile("global_load_dwordx2 %0, %1, %2" : "=&v"(d) : "v"(voff), "s"(base) : "memory");
}
DI void ldgh(u32& d, const char* base, u32 voff) {
  asm volatile("global_load_ushort %0, %1, %2" : "=&v"(d) : "v"(voff), "s"(base) : "memory");
}
DI void stc4(const char* base, u32 voff, u32x4 v) {
  asm volatile("global_store_dwordx4 %0, %1, %2 sc1\n\ts_nop 1" ::"v"(voff), "v"(v), "s"(base) : "memory");
}
DI void stc2(const char* base, u32 voff, u32x2 v) {
  asm volatile("global_store_dwordx2 %0, %1, %2 sc1" ::"v"(voff), "v"(v), "s"(base) : "memory");
}
DI void stc1(const char* base, u32 voff, u32 v) {
  asm volatile("global_store_dword %0, %1, %2 sc1" ::"v"(voff), "v"(v), "s"(base) : "memory");
}

DI u32x4 lr4(u32 a) { return *(LAS u32x4*)a; }
DI void lw4(u32 a, u32x4 v) { *(LAS u32x4*)a = v; }
DI void lw2(u32 a, u32x2 v) { *(LAS u32x2*)a = v; }
DI void lw1(u32 a, u32 v) { *(LAS u32*)a = v; }
DI u32x4 ltr(u32 a0, u32 a1) {
  u32x2 p = __builtin_bit_cast(u32x2, __builtin_amdgcn_ds_read_tr16_b64_v4i16((LAS s16x4*)a0));
  u32x2 q = __builtin_bit_cast(u32x2, __builtin_amdgcn_ds_read_tr16_b64_v4i16((LAS s16x4*)a1));
  return u32x4{p.x, p.y, q.x, q.y};
}
DI f32x4 mfma(u32x4 a, u32x4 b, f32x4 c) {
  return __builtin_amdgcn_mfma_f32_16x16x32_bf16(__builtin_bit_cast(bf16x8, a), __builtin_bit_cast(bf16x8, b), c, 0, 0, 0);
}
DI u32 fu(float f) { return __float_as_uint(f); }
DI float uf(u32 u) { return __uint_as_float(u); }
DI u32 pk(float a, float b) { return __builtin_bit_cast(u32, __builtin_convertvector(f32x2{a, b}, bf16x2)); }
DI float lo(u32 u) { return uf(u << 16); }
DI float hi(u32 u) { return uf(u & 0xffff0000u); }
DI float rbf(float a) { return lo(pk(a, a)); }
DI u32 canon(u32 u) { return u < SENT - 1 ? u : SENT - 1; }
DI u32x4 canon4(f32x4 v) { return u32x4{canon(fu(v.x)), canon(fu(v.y)), canon(fu(v.z)), canon(fu(v.w))}; }
#pragma float_control(push)
#pragma float_control(precise, off)
DI float fmx(float a, float b) { return __builtin_fmaxf(a, b); }
#pragma float_control(pop)
DI u32 umax(u32 a, u32 b) { return a > b ? a : b; }
DI u32 umax4(u32x4 v) { return umax(umax(v.x, v.y), umax(v.z, v.w)); }
template <int C> DI float ror(float v) { return uf((u32)__builtin_amdgcn_update_dpp(0, (int)fu(v), 0x120 + C, 0xf, 0xf, true)); }
template <int BANKS> DI u32 ror8m(u32 keep, u32 src) { return (u32)__builtin_amdgcn_update_dpp((int)keep, (int)src, 0x128, 0xf, BANKS, false); }
DI float add32(float a, float b) { auto s = __builtin_amdgcn_permlane32_swap(fu(a), fu(b), false, false); return uf(s[0]) + uf(s[1]); }
DI float add16(float a, float b) { auto s = __builtin_amdgcn_permlane16_swap(fu(a), fu(b), false, false); return uf(s[0]) + uf(s[1]); }
DI float max32(float a) { auto s = __builtin_amdgcn_permlane32_swap(fu(a), fu(a), false, false); return fmx(uf(s[0]), uf(s[1])); }
DI float max16(float a) { auto s = __builtin_amdgcn_permlane16_swap(fu(a), fu(a), false, false); return fmx(uf(s[0]), uf(s[1])); }
DI float wsum(float v) {
  v += ror<8>(v); v += ror<4>(v); v += ror<2>(v); v += ror<1>(v);
  v = add16(v, v);
  return add32(v, v);
}
DI f32x4 sel(bool c, f32x4 a, f32x4 b) { return f32x4{c ? a.x : b.x, c ? a.y : b.y, c ? a.z : b.z, c ? a.w : b.w}; }
DI void lds_signal(u32 a) {
  asm volatile("s_waitcnt lgkmcnt(0)" ::: "memory");
  lw1(a, 1u);
}
DI void lds_wait4(u32 a, u32& spins) {
  for (;;) {
    asm volatile("" ::: "memory");
    const u32x4 f = lr4(a);
    if (__builtin_amdgcn_readfirstlane(f.x & f.y & f.z & f.w) || ++spins > SPIN_MAX) break;
  }
  asm volatile("" ::: "memory");
}
DI bool none(bool bad) { return __builtin_amdgcn_ballot_w64(bad) == 0; }
DI float ex2(float v) { return __builtin_amdgcn_exp2f(v); }
DI float rcp(float v) { return __builtin_amdgcn_rcpf(v); }
template <int N> DI void nap() { if (N > 0) __builtin_amdgcn_s_sleep(N); }

DI void red8(const float (&a)[8], float& o0, float& o1) {
  float r0 = add32(a[0], a[4]), r1 = add32(a[1], a[5]), r2 = add32(a[2], a[6]), r3 = add32(a[3], a[7]);
  float s0 = add16(r0, r2), s1 = add16(r1, r3);
  o0 = s0 + ror<8>(s0);
  o1 = s1 + ror<8>(s1);
}
DI float red4(f32x4 a) {
  float r0 = add32(a.x, a.z), r1 = add32(a.y, a.w);
  float s = add16(r0, r1);
  return s + ror<8>(s);
}

#define STAMP(i) do { if ((i) == 0) at0 = (u32)__builtin_amdgcn_s_memrealtime(); if ((i) == AMP) at1 = (u32)__builtin_amdgcn_s_memrealtime(); } while (0)
#define STAMPW(i) STAMP(i)

__global__ __launch_bounds__(512, 2) void mla_ffn_decode(Args a) {
  const u32 tid = threadIdx.x, bid = blockIdx.x, lane = tid & 63;
  const u32 w = __builtin_amdgcn_readfirstlane(tid >> 6);
  u32 at0 = 0, at1 = 0;
  STAMP(0);
  const u32 xcd = bid & 7, r = bid >> 3, grp = r & 1, j = (r >> 1) & 7, h = grp * 16 + xcd * 2 + (r >> 4);
  const u32 ab = xcd >> 1, aks = (xcd & 1) * 16 + (r >> 1), hg = 1 - grp, sp = ab * 32 + aks;
  const u32 mq = j >> 1, dh = j & 1;
  const u32 l15 = lane & 15, g = lane >> 4, bq = lane & 3, u = (lane >> 2) & 3, l7 = lane & 7, l3 = lane >> 3;
  const u32 tq = u, tp = bq;
  const bool ffn = w == 3 && h < 28, fin = w == 7 && h >= 24;
  if (w == 0) lw2(L_FL + lane * 8, u32x2{0, 0});
  BARRIER();
  const char* ws = a.ws;
  const u32 tag = *(const __attribute__((address_space(4))) u32*)ws + 1;
  u32 spins = 0;

  const u32 f1 = (l3 >> 1) & 1;
  const u32 trn = (g * 8 + tq) * 128 + (tp & 1) * 8, trf = ((tq >> 1) | ((g & 1) << 1)) << 1;
  const u32x4 sent4 = {SENT, SENT, SENT, SENT};
  u32 o5, ohf, ost, o3, o3b, bfr, va, frag[4];
  u32x4 xf0, xf1;
#define COMMON_ADDR()                                                                                        \
  do {                                                                                                       \
    o5 = (j * 8 + bq * 2 + (u >> 1)) * 4096 + h * 128 + (u & 1) * 64 + g * 16;                               \
    ohf = W_HF + h * 8192 + j * 1024 + lane * 16;                                                            \
    ost = W_ST + (hg * 16 + l15) * 2048 + sp * 16;                                                           \
    o3 = W_H3 + (((hg * 16 + l7) * 8 + w) * 128 + sp) * 128 + (((lane >> 3) & 1) * 4 + g) * 16;              \
    o3b = o3 + 8 * 8 * 16384;                                                                                \
    bfr = g * 64 + bq * 16;                                                                                  \
    va = (g * 4 + tq) * 1024 + (((w * 8 + (tp >> 1)) ^ (((g & 1) * 4 + tq) << 1)) << 4) + (tp & 1) * 8;      \
    _Pragma("unroll") for (int i = 0; i < 4; ++i) frag[i] = (l15 * 1024 + ((g ^ (l7 << 1)) << 4)) ^ (i << 6); \
    PIN(o5); PIN(ohf); PIN(ost); PIN(o3); PIN(o3b); PIN(bfr); PIN(va);                                       \
    PIN(frag[0]); PIN(frag[1]); PIN(frag[2]); PIN(frag[3]);                                                  \
  } while (0)
  const char* kvb = a.ckv + (size_t)(ab * 4096 + aks * 128 + (w & 3) * 32) * 1024;

  if (w < 4) {
    u32x4 xv;
    ldg4(xv, a.x, w * 1024 + lane * 16);
    const u32 vq = ((mq * 128 + w * 32 + l3) * 32 + h) * 256 + dh * 128;
    const u32 vq0 = vq + ((l7 ^ (f1 << 1)) << 4), vq1 = vq + 8 * 8192 + ((l7 ^ ((f1 | 2) << 1)) << 4);
    dma<1>(a.wq, vq0, L_X + w * 4096);
    dma<1>(a.wq, vq1, L_X + w * 4096 + 1024);
    dma<1>(a.wq + 16 * 8192, vq0, L_X + w * 4096 + 2048);
    dma<1>(a.wq + 16 * 8192, vq1, L_X + w * 4096 + 3072);
#define UK_DMA()                                                                                              \
  _Pragma("unroll") for (int i = 0; i < 4; ++i)                                                               \
      dma<1>(a.wuk + i * 4 * 8192, ((j * 64 + w * 16 + g) * 32 + h) * 256 + ((l15 ^ (i * 4 + g)) << 4), L_Y + w * 4096 + i * 1024)
    if (!P_UKL) { UK_DMA(); }
    COMMON_ADDR();
    u32 oc = w == 0 ? ohf : w == 1 ? W_H4 + h * 8192 + l3 * 1024 + j * 128 + l7 * 16 : w == 2 ? W_H5 + o5 : W_HY + o5;
    u32 taq = L_X + trn + (((2 * w + (tp >> 1)) ^ trf) << 4);
    u32 o1s = W_H1 + h * 16384 + dh * 8192 + w * 2048 + mq * 512 + (g * 8 + l7) * 16;
    u32 o1 = W_H1 + h * 16384 + w * 4096 + lane * 16;
    u32 iq = L_Q0 + (4 * w + (lane >> 5) * 2 + (g & 1)) * 64 + bq * 16 + (l3 & 1) * 8 + (u & 1) * 4;
    u32 ua = L_Y + (w * 32 + l15) * 256 + ((g ^ l15) << 4);
    u32 o2s = W_H2 + (bq * 32 + h) * 2048 + j * 256 + w * 128 + u * 64 + g * 16;
    u32 o2 = W_H2 + (ab * 32 + hg * 16 + 4 * w) * 2048 + lane * 16, o2b = o2 + 4096;
    u32 i0 = L_X + 4 * w * 1024 + (((lane >> 1) ^ (((4 * w) & 7) << 1)) << 4) + (lane & 1) * 8;
    PIN(oc); PIN(taq); PIN(o1s); PIN(o1); PIN(iq); PIN(ua); PIN(o2s); PIN(o2); PIN(o2b); PIN(i0);

    WAITV(P_UKL ? 4 : 8);
    STAMP(19);
    PIN(xv);
    {
      float v[8] = {lo(xv.x), hi(xv.x), lo(xv.y), hi(xv.y), lo(xv.z), hi(xv.z), lo(xv.w), hi(xv.w)};
      const float mean = wsum(((v[0] + v[1]) + (v[2] + v[3])) + ((v[4] + v[5]) + (v[6] + v[7]))) * (1.f / 512.f);
      float d[8];
#pragma unroll
      for (int i = 0; i < 8; ++i) { d[i] = v[i] - mean; d[i] *= d[i]; }
      const float var = rbf(wsum(((d[0] + d[1]) + (d[2] + d[3])) + ((d[4] + d[5]) + (d[6] + d[7]))) * (1.f / 512.f)), mb = rbf(mean);
      const float inv = rcp(rbf(__builtin_amdgcn_sqrtf(rbf(var + 1e-5f))));
      u32x4 o;
#pragma unroll
      for (int i = 0; i < 4; ++i) { const u32 c = pk(v[2 * i] - mb, v[2 * i + 1] - mb); o[i] = pk(lo(c) * inv, hi(c) * inv); }
      PIN(o);
      if (g == mq) lw4(L_XN + l15 * 64 + w * 16, o);
    }
    WAITV(P_UKL ? 0 : 4);
    STAMP(1);
    lds_signal(L_FL + w * 4);
    if (P_UKL == 1) { UK_DMA(); }
    lds_wait4(L_FL, spins);
    {
      u32x4 fa[4], fb[4];
#pragma unroll
      for (int ks = 0; ks < 4; ++ks) { fa[ks] = ltr(taq + ks * 4096, taq + ks * 4096 + 512); fb[ks] = lr4(L_XN + ks * 256 + bfr); }
      FENCE();
      f32x4 c0 = mfma(fa[0], fb[0], f32x4{0, 0, 0, 0}), c1 = mfma(fa[1], fb[1], f32x4{0, 0, 0, 0});
      c0 = mfma(fa[2], fb[2], c0);
      c1 = mfma(fa[3], fb[3], c1);
      xf0 = dh ? fb[2] : fb[0];
      xf1 = dh ? fb[3] : fb[1];
      const f32x4 acc = c0 + c1;
      u32x4 o = {fu((u & 1) ? acc.z : acc.x), tag, fu((u & 1) ? acc.w : acc.y), tag};
      PIN(o);
      if (u < 2) stc4(ws, o1s, o);
    }
    STAMP(2);
    if (P_ST == 1) lw1(L_FL + 32 + w * 4, 1u);
    if (P_UKL == 2) { UK_DMA(); }
    stc4(ws, oc, sent4);
    {
      u32x4 p0, p1, p2, p3;
      nap<P_D1>();
      for (;;) {
        ldc4<0>(p0, ws, o1); ldc4<1024>(p1, ws, o1); ldc4<2048>(p2, ws, o1); ldc4<3072>(p3, ws, o1);
        if (P_ST == 2) lw1(L_FL + 32 + w * 4, 1u);
        WAITV(0);
        PIN(p0); PIN(p1); PIN(p2); PIN(p3);
        const u32 bad = (p0.y ^ tag) | (p0.w ^ tag) | (p1.y ^ tag) | (p1.w ^ tag) | (p2.y ^ tag) | (p2.w ^ tag) | (p3.y ^ tag) | (p3.w ^ tag);
        if (none(bad != 0) || ++spins > SPIN_MAX) break;
      }
      lw1(iq, pk(add32(uf(p0.x) + uf(p1.x), uf(p2.x) + uf(p3.x)), add32(uf(p0.z) + uf(p1.z), uf(p2.z) + uf(p3.z))));
    }
    STAMP(3);
    if (P_ST == 3) lw1(L_FL + 32 + w * 4, 1u);
    lds_signal(L_FL + 16 + w * 4);
    if (w < 2) {
      u32x4 fb[4], f0[4], f1v[4];
      lds_wait4(L_FL + 16, spins);
#pragma unroll
      for (int ks = 0; ks < 4; ++ks) { fb[ks] = lr4(L_Q0 + bfr + ks * 256); f0[ks] = lr4(ua ^ (ks << 6)); f1v[ks] = lr4((ua ^ (ks << 6)) + 4096); }
      FENCE();
      f32x4 c0 = {0, 0, 0, 0}, c1 = {0, 0, 0, 0};
#pragma unroll
      for (int ks = 0; ks < 4; ++ks) { c0 = mfma(f0[ks], fb[ks], c0); c1 = mfma(f1v[ks], fb[ks], c1); }
      const float sc = 0.08838834764831845f * 1.4426950408889634f;
      const f32x4 c = sel(u & 1, c1, c0) * sc;
      u32x4 o = {pk(c.x, c.y), tag, pk(c.z, c.w), tag};
      PIN(o);
      if (u < 2) stc4(ws, o2s, o);
      STAMP(4);
      nap<P_D2>();
    } else {
      lds_wait4(L_FL + 16, spins);
      nap<P_D2 + 6>();
    }
    u32x4 s[8];
    for (;;) {
      ldc4<0>(s[0], ws, o2); ldc4<1024>(s[1], ws, o2); ldc4<2048>(s[2], ws, o2); ldc4<3072>(s[3], ws, o2);
      ldc4<0>(s[4], ws, o2b); ldc4<1024>(s[5], ws, o2b); ldc4<2048>(s[6], ws, o2b); ldc4<3072>(s[7], ws, o2b);
      WAITV(0);
      u32 bad = 0;
#pragma unroll
      for (int i = 0; i < 8; ++i) { PIN(s[i]); bad |= (s[i].y ^ tag) | (s[i].w ^ tag); }
      if (none(bad != 0) || ++spins > SPIN_MAX) break;
    }
#pragma unroll
    for (int i = 0; i < 8; ++i) lw2((i0 ^ ((i >> 1) << 5)) + (i >> 1) * 1024 + (i & 1) * 512, u32x2{s[i].x, s[i].z});
    STAMP(5);
    BARRIER();
  } else {
    COMMON_ADDR();
    if (P_ST) lds_wait4(L_FL + 32, spins);
    nap<P_DS>();
    STAMP(2);
    const u32 lb = (w & 3) * 32768, off = grp ? P_OFF : 0;
#pragma unroll
    for (int i = 0; i < 31; ++i) {
      if (i >= P_W) WAITV(P_W - 1);
      const u32 row = i < 31 - P_OFF ? i + off : i - (31 - P_OFF) + (grp ? 0 : 31 - P_OFF);
      dma<KVNT>(kvb + row * 1024, (lane ^ ((row & 7) << 1)) << 4, lb + row * 1024);
    }
    STAMP(4);
    BARRIER();
    dma<KVNT>(kvb + 31 * 1024, (lane ^ 14) << 4, lb + 31 * 1024);
  }
  stc4(ws, o3, sent4);
  stc4(ws, o3b, sent4);
  const u32 lkv = w * 16384;
  const u32 vuk = ((j * 64 + w * 8 + g) * 32 + h) * 256;
  dma<1>(a.wuv, vuk + ((l15 ^ (g << 1)) << 4), L_Y + w * 2048);
  dma<1>(a.wuv, vuk + 4 * 8192 + ((l15 ^ ((4 + g) << 1)) << 4), L_Y + w * 2048 + 1024);

  u32x4 fr[16];
#pragma unroll
  for (int ks = 0; ks < 16; ++ks) fr[ks] = lr4(L_X + frag[ks & 3] + (ks >> 2) * 256);
#pragma unroll
  for (int i = 0; i < 4; ++i) { frag[i] += lkv; PIN(frag[i]); }
  STAMP(6);
  BARRIER();
  if (ffn) {
#pragma unroll
    for (int i = 0; i < 8; ++i)
      dma<1>(a.w1 + i * 8 * 3584, (j * 64 + l3) * 3584 + h * 128 + ((l7 ^ ((f1 | ((i & 1) << 1)) << 1)) << 4), L_W1 + i * 1024);
  }
  u32 imx = L_MX + l15 * 32, ip = L_P + lane * 16;
  PIN(imx); PIN(ip);
  if (w >= 4) WAITV(4);
  STAMP(7);
  BARRIER();

  f32x4 sv;
  {
    u32x4 fa[16];
#pragma unroll
    for (int ks = 0; ks < 16; ++ks) fa[ks] = lr4(frag[ks & 3] + (ks >> 2) * 256);
    FENCE();
    f32x4 c0 = {0, 0, 0, 0}, c1 = {0, 0, 0, 0};
#pragma unroll
    for (int ks = 0; ks < 16; ks += 2) { c0 = mfma(fa[ks], fr[ks], c0); c1 = mfma(fa[ks + 1], fr[ks + 1], c1); }
    sv = c0 + c1;
    float m = max32(max16(fmx(fmx(sv.x, sv.y), fmx(sv.z, sv.w))));
    PIN(m);
    if (g == 0) lw1(imx + w * 4, fu(m));
  }
  STAMP(8);
  BARRIER();
  float mx;
  {
    const u32x4 m0 = lr4(imx), m1 = lr4(imx + 16);
    FENCE();
#pragma unroll
    for (int ks = 0; ks < 4; ++ks)
#pragma unroll
      for (int t = 0; t < 4; ++t) fr[t * 4 + ks] = ltr((va ^ (t << 5)) + ks * 32768, (va ^ (t << 5)) + ks * 32768 + 16384);
    FENCE();
    mx = fmx(fmx(fmx(uf(m0.x), uf(m0.y)), fmx(uf(m0.z), uf(m0.w))), fmx(fmx(uf(m1.x), uf(m1.y)), fmx(uf(m1.z), uf(m1.w))));
    const u32 p0 = pk(ex2(sv.x - mx), ex2(sv.y - mx)), p1 = pk(ex2(sv.z - mx), ex2(sv.w - mx));
    lw2(ip + (w >> 1) * 1024 + (w & 1) * 8, u32x2{p0, p1});
    float l = (lo(p0) + hi(p0)) + (lo(p1) + hi(p1));
    l = add16(l, l);
    l = add32(l, l);
    PIN(l);
    if (g == 0) lw1(imx + (L_LS - L_MX) + w * 4, fu(l));
    if (ffn) WAITV(10); else WAITV(2);
  }
  STAMP(9);
  BARRIER();
#define WO_DMA()                                                                          \
  do {                                                                                    \
    if (ffn) {                                                                            \
      _Pragma("unroll") for (int i = 0; i < 8; ++i)                                       \
          dma<1>(a.w2 + i * 8192, (h * 64 + l3) * 1024 + j * 128 + ((l7 ^ (l3 & 6)) << 4), L_W2 + i * 1024); \
    }                                                                                     \
    const u32 vo = (h * 128 + w * 16 + l3) * 1024 + j * 128;                              \
    dma<1>(a.wo, vo + ((l7 ^ (f1 << 1)) << 4), L_WO + w * 2048);                          \
    dma<1>(a.wo, vo + 8 * 1024 + ((l7 ^ ((f1 | 2) << 1)) << 4), L_WO + w * 2048 + 1024);  \
  } while (0)
  {
    u32x4 pf[4];
#pragma unroll
    for (int ks = 0; ks < 4; ++ks) pf[ks] = lr4(ip + ks * 1024);
    if (w == 7) {
      const u32x4 l0 = lr4(imx + (L_LS - L_MX)), l1 = lr4(imx + (L_LS - L_MX) + 16);
      u32x4 o = {fu(mx), tag, fu(((uf(l0.x) + uf(l0.y)) + (uf(l0.z) + uf(l0.w))) + ((uf(l1.x) + uf(l1.y)) + (uf(l1.z) + uf(l1.w)))), tag};
      PIN(o);
      if (lane < 16) stc4(ws, ost, o);
    }
    FENCE();
    f32x4 o[4] = {{0, 0, 0, 0}, {0, 0, 0, 0}, {0, 0, 0, 0}, {0, 0, 0, 0}};
#pragma unroll
    for (int ks = 0; ks < 4; ++ks)
#pragma unroll
      for (int t = 0; t < 4; ++t) o[t] = mfma(fr[t * 4 + ks], pf[ks], o[t]);
    const u32x4 r0 = {pk(o[0].x, o[0].y), pk(o[0].z, o[0].w), pk(o[1].x, o[1].y), pk(o[1].z, o[1].w)};
    const u32x4 r1 = {pk(o[2].x, o[2].y), pk(o[2].z, o[2].w), pk(o[3].x, o[3].y), pk(o[3].z, o[3].w)};
    u32x4 n0, n1;
#pragma unroll
    for (int e = 0; e < 4; ++e) { n0[e] = canon(ror8m<0xC>(r0[e], r1[e])); n1[e] = canon(ror8m<0x3>(r1[e], r0[e])); }
    stc4(ws, o3, n0);
    stc4(ws, o3b, n1);
  }
  STAMP(10);
  if (P_WO == 0) WO_DMA();

  const u32 fb = (h >> 1) & 3, fm = j * 64 + (h & 1) * 32 + l7 * 4 + (lane >> 5) * 2 + ((lane >> 4) & 1);
  u32 osn = W_ST + h * 2048 + ((w >> 1) * 32 + l3) * 16 + (w & 1) * 256, oso = osn ^ 256;
  u32 od = W_H3 + ((h * 8 + j) * 128 + w * 16) * 128 + lane * 16;
  u32 icb = L_CB + l7 * 128 + ((((w & 1) * 4 + (w >> 1)) ^ l7) << 4) + (lane >> 5) * 8 + ((lane >> 4) & 1) * 4;
  u32 vu = L_Y + (g * 4 + tq) * 256 + (((2 * w + (tp >> 1)) ^ (((g & 1) * 4 + tq) << 1)) << 4) + (tp & 1) * 8;
  u32 rcb = L_CB + g * 128 + ((l7 ^ g) << 4);
  u32 o4s = W_H4 + h * 8192 + w * 1024 + j * 128 + bq * 32 + g * 8;
  PIN(osn); PIN(oso); PIN(od); PIN(icb); PIN(vu); PIN(rcb); PIN(o4s);
  if (ffn) {
    WAITV(P_WO == 0 ? 12 : 2);
    u32x4 fa[8];
#pragma unroll
    for (int t = 0; t < 4; ++t) {
      const u32 ta = L_W1 + trn + (((2 * t + (tp >> 1)) ^ trf) << 4);
      fa[2 * t] = ltr(ta, ta + 512);
      fa[2 * t + 1] = ltr(ta + 4096, ta + 4096 + 512);
    }
    FENCE();
    f32x4 d[4];
#pragma unroll
    for (int t = 0; t < 4; ++t) d[t] = mfma(fa[2 * t], xf0, f32x4{0, 0, 0, 0});
#pragma unroll
    for (int t = 0; t < 4; ++t) d[t] = mfma(fa[2 * t + 1], xf1, d[t]);
    stc4(ws, ohf, canon4(sel(u & 2, sel(u & 1, d[3], d[2]), sel(u & 1, d[1], d[0]))));
    nap<(P_D3 > 10 ? P_D3 - 10 : 0)>();
  } else {
    nap<P_D3>();
  }
  {
    u32x4 e0, e1, e2, e3, d0, d1;
    STAMP(11);
    for (;;) {
      ldc4<0>(e0, ws, osn); ldc4<128>(e1, ws, osn); ldc4<0>(e2, ws, oso); ldc4<128>(e3, ws, oso);
      WAITV(0);
      PIN(e0); PIN(e1); PIN(e2); PIN(e3);
      const u32 bad = (e0.y ^ tag) | (e0.w ^ tag) | (e1.y ^ tag) | (e1.w ^ tag) | (e2.y ^ tag) | (e2.w ^ tag) | (e3.y ^ tag) | (e3.w ^ tag);
      if (none(bad != 0) || ++spins > SPIN_MAX) break;
    }
    STAMP(17);
    nap<P_D3B>();
    ldc4<0>(d0, ws, od); ldc4<1024>(d1, ws, od);
    if (P_WO == 1) WO_DMA();
    float m = fmx(fmx(uf(e0.x), uf(e1.x)), fmx(uf(e2.x), uf(e3.x)));
    m = max32(max16(fmx(m, ror<8>(m))));
    float w0 = ex2(uf(e0.x) - m), w1 = ex2(uf(e1.x) - m);
    float l = w0 * uf(e0.z) + w1 * uf(e1.z) + ex2(uf(e2.x) - m) * uf(e2.z) + ex2(uf(e3.x) - m) * uf(e3.z);
    l += ror<8>(l);
    l = add16(l, l);
    l = add32(l, l);
    const float il = rcp(l);
    w0 *= il; w1 *= il;
    PIN(w0); PIN(w1);
    if (P_WO != 1) WAITV(0); else if (ffn) WAITV(10); else WAITV(2);
    for (;;) {
      PIN(d0); PIN(d1);
      if (none(umax(umax4(d0), umax4(d1)) == SENT) || ++spins > SPIN_MAX) break;
      ldc4<0>(d0, ws, od); ldc4<1024>(d1, ws, od);
      WAITV(0);
    }
    STAMP(12);
    if (P_WO == 2) WO_DMA();
    float acc[8];
#pragma unroll
    for (int e = 0; e < 4; ++e) {
      acc[2 * e] = w0 * lo(d0[e]) + w1 * lo(d1[e]);
      acc[2 * e + 1] = w0 * hi(d0[e]) + w1 * hi(d1[e]);
    }
    float c0, c1;
    red8(acc, c0, c1);
    u32 o = pk(c0, c1);
    PIN(o);
    if (!(lane & 8)) lw1(icb, o);
  }
  STAMP(13);
  BARRIER();
  {
    const u32x4 a0 = ltr(vu, vu + 4096), b0 = lr4(rcb), a1 = ltr(vu + 8192, vu + 8192 + 4096), b1 = lr4((rcb ^ 64) + 512);
    FENCE();
    const f32x4 c = mfma(a0, b0, f32x4{0, 0, 0, 0}) + mfma(a1, b1, f32x4{0, 0, 0, 0});
    u32x2 v = {canon(pk(c.x + ror<4>(c.x), c.y + ror<4>(c.y))), canon(pk(c.z + ror<4>(c.z), c.w + ror<4>(c.w)))};
    PIN(v);
    if (u == 0) stc2(ws, o4s, v);
  }
  STAMP(14);
  if (P_WO != 0) { WAITV(1); BARRIER(); }
  if (w < 2) {
    const u32 t0 = L_WO + trn + (((4 * w + (tp >> 1)) ^ trf) << 4), t1 = t0 ^ 32;
    u32 o4 = W_H4 + h * 8192 + (g >> 1) * 1024 + u * 128 + bq * 32 + (g & 1) * 16, o4b = o4 + 4096;
    u32 o5s = W_H5 + (j * 8 + bq * 2 + w) * 4096 + h * 128 + u * 64 + g * 16;
    u32x4 fa[8];
#pragma unroll
    for (int ks = 0; ks < 4; ++ks) { fa[ks] = ltr(t0 + ks * 4096, t0 + ks * 4096 + 512); fa[4 + ks] = ltr(t1 + ks * 4096, t1 + ks * 4096 + 512); }
#pragma unroll
    for (int i = 0; i < 8; ++i) PIN(fa[i]);
    PIN(o4); PIN(o4b); PIN(o5s);
    nap<P_D4>();
    u32x4 p[8];
    for (;;) {
      ldc4<0>(p[0], ws, o4); ldc4<512>(p[1], ws, o4); ldc4<2048>(p[2], ws, o4); ldc4<2560>(p[3], ws, o4);
      ldc4<0>(p[4], ws, o4b); ldc4<512>(p[5], ws, o4b); ldc4<2048>(p[6], ws, o4b); ldc4<2560>(p[7], ws, o4b);
      WAITV(0);
      u32 t = 0;
#pragma unroll
      for (int i = 0; i < 8; ++i) { PIN(p[i]); t = umax(t, umax4(p[i])); }
      if (none(t == SENT) || ++spins > SPIN_MAX) break;
    }
    STAMP(15);
    f32x4 c[4] = {{0, 0, 0, 0}, {0, 0, 0, 0}, {0, 0, 0, 0}, {0, 0, 0, 0}};
#pragma unroll
    for (int ks = 0; ks < 4; ++ks) {
      c[0] = mfma(fa[ks], p[2 * ks], c[0]);
      c[1] = mfma(fa[4 + ks], p[2 * ks], c[1]);
      c[2] = mfma(fa[ks], p[2 * ks + 1], c[2]);
      c[3] = mfma(fa[4 + ks], p[2 * ks + 1], c[3]);
    }
    f32x4 c0 = c[0] + c[2], c1 = c[1] + c[3];
#pragma unroll
    for (int e = 0; e < 4; ++e) {
      c0[e] += ror<8>(c0[e]); c1[e] += ror<8>(c1[e]);
      c0[e] += ror<4>(c0[e]); c1[e] += ror<4>(c1[e]);
    }
    u32x4 o = canon4(sel(u & 1, c1, c0));
    PIN(o);
    if (u < 2) stc4(ws, o5s, o);
  } else if (ffn) {
    const u32 of = W_HF + h * 8192 + lane * 16;
    const u32 f2 = (((g & 1) << 1) | (tq >> 1)) << 1;
    u32x4 p[8], fa[8];
    u32x2 b1v;
    ldg2(b1v, a.b1, (h * 64 + u * 16 + g * 4) * 2);
    for (;;) {
      ldc4<0>(p[0], ws, of); ldc4<1024>(p[1], ws, of); ldc4<2048>(p[2], ws, of); ldc4<3072>(p[3], ws, of);
      ldc4<0>(p[4], ws, of + 4096); ldc4<1024>(p[5], ws, of + 4096); ldc4<2048>(p[6], ws, of + 4096); ldc4<3072>(p[7], ws, of + 4096);
#pragma unroll
      for (int t = 0; t < 4; ++t) {
        const u32 ta = L_W2 + (g * 4 + tq) * 128 + (((2 * t + (tp >> 1)) ^ f2) << 4) + (tp & 1) * 8;
        fa[2 * t] = ltr(ta, ta + 2048);
        fa[2 * t + 1] = ltr(ta + 4096, ta + 4096 + 2048);
      }
      WAITV(0);
      u32 t = 0;
#pragma unroll
      for (int i = 0; i < 8; ++i) { PIN(p[i]); t = umax(t, umax4(p[i])); }
      if (none(t == SENT) || ++spins > SPIN_MAX) break;
    }
    PIN(b1v);
    const float bb[4] = {lo(b1v.x), hi(b1v.x), lo(b1v.y), hi(b1v.y)};
    float ge[4];
#pragma unroll
    for (int e = 0; e < 4; ++e) {
      const float s = ((uf(p[0][e]) + uf(p[1][e])) + (uf(p[2][e]) + uf(p[3][e]))) + ((uf(p[4][e]) + uf(p[5][e])) + (uf(p[6][e]) + uf(p[7][e])));
      const float t = rbf(rbf(s) + bb[e]);
      const float z = ex2(2.302208198f * (t + 0.044715f * t * t * t));
      ge[e] = t - t * rcp(z + 1.f);
    }
    lw2(L_HB + (u >> 1) * 256 + bfr + (u & 1) * 8, u32x2{pk(ge[0], ge[1]), pk(ge[2], ge[3])});
    asm volatile("s_waitcnt lgkmcnt(0)" ::: "memory");
    const u32x4 hb0 = lr4(L_HB + bfr), hb1 = lr4(L_HB + 256 + bfr);
    f32x4 d[4];
#pragma unroll
    for (int t = 0; t < 4; ++t) d[t] = mfma(fa[2 * t], hb0, f32x4{0, 0, 0, 0});
#pragma unroll
    for (int t = 0; t < 4; ++t) d[t] = mfma(fa[2 * t + 1], hb1, d[t]);
    stc4(ws, W_HY + o5, canon4(sel(u & 2, sel(u & 1, d[3], d[2]), sel(u & 1, d[1], d[0]))));
    STAMP(15);
  } else if (fin) {
    u32 fy = W_HY + (j * 8 + (h & 7)) * 4096 + lane * 16, fy3 = lane < 32 ? fy + 3072 : fy, fs = fy + (W_H5 - W_HY);
    u32 oo = (fb * 512 + fm) * 2;
    u32 xo, b2o;
    ldgh(xo, a.x, oo);
    ldgh(b2o, a.b2, fm * 2);
    PIN(fy); PIN(fy3); PIN(fs); PIN(oo);
    nap<P_D5>();
    u32x4 p0, p1, p2, p3;
    for (;;) {
      ldc4<0>(p0, ws, fy); ldc4<1024>(p1, ws, fy); ldc4<2048>(p2, ws, fy); ldc4<0>(p3, ws, fy3);
      WAITV(0);
      PIN(p0); PIN(p1); PIN(p2); PIN(p3);
      if (none(umax(umax(umax4(p0), umax4(p1)), umax(umax4(p2), umax4(p3))) == SENT) || ++spins > SPIN_MAX) break;
    }
    STAMP(15);
    u32x4 q0, q1, q2, q3;
    ldc4<0>(q0, ws, fs); ldc4<1024>(q1, ws, fs); ldc4<2048>(q2, ws, fs); ldc4<3072>(q3, ws, fs);
    PIN(xo); PIN(b2o);
    const f32x4 z = {0, 0, 0, 0};
    const f32x4 sy = __builtin_bit_cast(f32x4, p0) + __builtin_bit_cast(f32x4, p1) + __builtin_bit_cast(f32x4, p2) + sel(lane < 32, __builtin_bit_cast(f32x4, p3), z);
    const float y = rbf(rbf(red4(sy)) + lo(b2o));
    float gate = rbf(y * rcp(1.f + ex2(-1.4426950408889634f * y)));
    PIN(gate);
    WAITV(0);
    for (;;) {
      PIN(q0); PIN(q1); PIN(q2); PIN(q3);
      if (none(umax(umax(umax4(q0), umax4(q1)), umax(umax4(q2), umax4(q3))) == SENT) || ++spins > SPIN_MAX) break;
      ldc4<0>(q0, ws, fs); ldc4<1024>(q1, ws, fs); ldc4<2048>(q2, ws, fs); ldc4<3072>(q3, ws, fs);
      WAITV(0);
    }
    const f32x4 s = (__builtin_bit_cast(f32x4, q0) + __builtin_bit_cast(f32x4, q1)) + (__builtin_bit_cast(f32x4, q2) + __builtin_bit_cast(f32x4, q3));
    const float o = lo(xo) + rbf(rbf(red4(s)) * gate);
    if (!(lane & 8)) *(u16*)(a.out + oo) = (u16)pk(o, o);
    if (h == 24 && j == 0 && lane == 0) stc1(ws, 0, tag);
  }
  STAMP(18);
  if (fin) { const u32 t = (u32)__builtin_amdgcn_s_memrealtime(), d = (at1 - at0) * 8; while ((u32)__builtin_amdgcn_s_memrealtime() - t < (d < 40000 ? d : 40000)) {} }
}

extern "C" void kernel_launch(void* const* d_in, const int* in_sizes, int n_in, void* d_out, int out_size, void* d_ws, size_t ws_size, hipStream_t stream) {
  Args a;
  a.x = (const char*)d_in[0];
  a.ckv = (const char*)d_in[1];
  a.wq = (const char*)d_in[2];
  a.wuk = (const char*)d_in[3];
  a.wuv = (const char*)d_in[4];
  a.wo = (const char*)d_in[5];
  a.w1 = (const char*)d_in[7];
  a.b1 = (const char*)d_in[8];
  a.w2 = (const char*)d_in[9];
  a.b2 = (const char*)d_in[10];
  a.out = (char*)d_out;
  a.ws = (char*)d_ws;
  (void)hipFuncSetAttribute((const void*)mla_ffn_decode, hipFuncAttributeMaxDynamicSharedMemorySize, L_TOTAL);
  mla_ffn_decode<<<256, 512, L_TOTAL, stream>>>(a);
}
```

```cpp
#define PRB 0
#include <hip/hip_runtime.h>
#include <hip/hip_bf16.h>
#include <stdint.h>

typedef unsigned u32;
typedef unsigned short u16;
typedef u32 u32x2 __attribute__((ext_vector_type(2)));
typedef u32 u32x4 __attribute__((ext_vector_type(4)));
typedef float f32x2 __attribute__((ext_vector_type(2)));
typedef float f32x4 __attribute__((ext_vector_type(4)));
typedef __bf16 bf16x2 __attribute__((ext_vector_type(2)));
typedef __bf16 bf16x8 __attribute__((ext_vector_type(8)));
typedef short s16x4 __attribute__((ext_vector_type(4)));
#define LAS __attribute__((address_space(3)))
#define DI __device__ __forceinline__

#define P_W 6
#define P_ST 1
#define P_OFF 0
#define P_DS 0
#define KVNT 0
#define P_D1 12
#define P_D2 14
#define P_D3 8
#define P_D4 12
#define P_D5 40
#define P_WO 1
#define P_UKL 1
#define P_D3B 0
constexpr u32 SPIN_MAX = 1u << 16;
constexpr u32 SENT = 0xFFFFFFFFu;

constexpr u32 W_H1 = 0x1000, W_H2 = W_H1 + 0x80000, W_ST = W_H2 + 0x40000, W_HF = W_ST + 0x10000;
constexpr u32 W_H4 = W_HF + 0x40000, W_H5 = W_H4 + 0x40000, W_HY = W_H5 + 0x40000, W_H3 = W_HY + 0x40000;
constexpr u32 W_DG = W_H3 + 0x400000;

constexpr u32 L_X = 131072, L_Y = L_X + 16384, L_TOTAL = L_Y + 16384;
constexpr u32 L_Q0 = 31 * 1024, L_XN = 63 * 1024, L_FL = 95 * 1024;
constexpr u32 L_W1 = L_X, L_MX = L_X + 8192, L_LS = L_X + 8704, L_P = L_X + 9216, L_CB = L_X + 13312, L_CX = L_X + 14336, L_HB = L_X + 15360;
constexpr u32 L_WO = 0, L_W2 = 16384;

struct Args {
  const char *x, *ckv, *wq, *wuk, *wuv, *wo, *w1, *b1, *w2, *b2;
  char *out, *ws;
};

#define WAITV(n) asm volatile("s_waitcnt vmcnt(%0)" ::"i"(n) : "memory")
#define BARRIER() asm volatile("s_waitcnt lgkmcnt(0)\n\ts_barrier" ::: "memory")
#define PIN(v) asm volatile("" : "+v"(v))
#define FENCE() __builtin_amdgcn_sched_barrier(0)

template <int NT> DI void dma(const char* base, u32 voff, u32 lds) {
  u32 keep;
  lds = __builtin_amdgcn_readfirstlane(lds);
  if (NT)
    asm volatile("s_mov_b32 %0, m0\n\ts_mov_b32 m0, %3\n\ts_nop 0\n\tglobal_load_lds_dwordx4 %1, %2 nt\n\ts_mov_b32 m0, %0"
                 : "=&s"(keep) : "v"(voff), "s"(base), "s"(lds) : "memory");
  else
    asm volatile("s_mov_b32 %0, m0\n\ts_mov_b32 m0, %3\n\ts_nop 0\n\tglobal_load_lds_dwordx4 %1, %2\n\ts_mov_b32 m0, %0"
                 : "=&s"(keep) : "v"(voff), "s"(base), "s"(lds) : "memory");
}
template <int OFF> DI void ldc4(u32x4& d, const char* base, u32 voff) {
  asm volatile("global_load_dwordx4 %0, %1, %2 offset:%3 sc1" : "=&v"(d) : "v"(voff), "s"(base), "i"(OFF) : "memory");
}
template <int OFF> DI void ldc2(u32x2& d, const char* base, u32 voff) {
  asm volatile("global_load_dwordx2 %0, %1, %2 offset:%3 sc1" : "=&v"(d) : "v"(voff), "s"(base), "i"(OFF) : "memory");
}
DI void ldg4(u32x4& d, const char* base, u32 voff) {
  asm volatile("global_load_dwordx4 %0, %1, %2" : "=&v"(d) : "v"(voff), "s"(base) : "memory");
}
DI void ldg2(u32x2& d, const char* base, u32 voff) {
  asm volatile("global_load_dwordx2 %0, %1, %2" : "=&v"(d) : "v"(voff), "s"(base) : "memory");
}
DI void ldgh(u32& d, const char* base, u32 voff) {
  asm volatile("global_load_ushort %0, %1, %2" : "=&v"(d) : "v"(voff), "s"(base) : "memory");
}
DI void stc4(const char* base, u32 voff, u32x4 v) {
  asm volatile("global_store_dwordx4 %0, %1, %2 sc1\n\ts_nop 1" ::"v"(voff), "v"(v), "s"(base) : "memory");
}
DI void stc2(const char* base, u32 voff, u32x2 v) {
  asm volatile("global_store_dwordx2 %0, %1, %2 sc1" ::"v"(voff), "v"(v), "s"(base) : "memory");
}
DI void stc1(const char* base, u32 voff, u32 v) {
  asm volatile("global_store_dword %0, %1, %2 sc1" ::"v"(voff), "v"(v), "s"(base) : "memory");
}

DI u32x4 lr4(u32 a) { return *(LAS u32x4*)a; }
DI void lw4(u32 a, u32x4 v) { *(LAS u32x4*)a = v; }
DI void lw2(u32 a, u32x2 v) { *(LAS u32x2*)a = v; }
DI void lw1(u32 a, u32 v) { *(LAS u32*)a = v; }
DI u32x4 ltr(u32 a0, u32 a1) {
  u32x2 p = __builtin_bit_cast(u32x2, __builtin_amdgcn_ds_read_tr16_b64_v4i16((LAS s16x4*)a0));
  u32x2 q = __builtin_bit_cast(u32x2, __builtin_amdgcn_ds_read_tr16_b64_v4i16((LAS s16x4*)a1));
  return u32x4{p.x, p.y, q.x, q.y};
}
DI f32x4 mfma(u32x4 a, u32x4 b, f32x4 c) {
  return __builtin_amdgcn_mfma_f32_16x16x32_bf16(__builtin_bit_cast(bf16x8, a), __builtin_bit_cast(bf16x8, b), c, 0, 0, 0);
}
DI u32 fu(float f) { return __float_as_uint(f); }
DI float uf(u32 u) { return __uint_as_float(u); }
DI u32 pk(float a, float b) { return __builtin_bit_cast(u32, __builtin_convertvector(f32x2{a, b}, bf16x2)); }
DI float lo(u32 u) { return uf(u << 16); }
DI float hi(u32 u) { return uf(u & 0xffff0000u); }
DI float rbf(float a) { return lo(pk(a, a)); }
DI u32 canon(u32 u) { return u < SENT - 1 ? u : SENT - 1; }
DI u32x4 canon4(f32x4 v) { return u32x4{canon(fu(v.x)), canon(fu(v.y)), canon(fu(v.z)), canon(fu(v.w))}; }
#pragma float_control(push)
#pragma float_control(precise, off)
DI float fmx(float a, float b) { return __builtin_fmaxf(a, b); }
#pragma float_control(pop)
DI u32 umax(u32 a, u32 b) { return a > b ? a : b; }
DI u32 umax4(u32x4 v) { return umax(umax(v.x, v.y), umax(v.z, v.w)); }
template <int C> DI float ror(float v) { return uf((u32)__builtin_amdgcn_update_dpp(0, (int)fu(v), 0x120 + C, 0xf, 0xf, true)); }
template <int BANKS> DI u32 ror8m(u32 keep, u32 src) { return (u32)__builtin_amdgcn_update_dpp((int)keep, (int)src, 0x128, 0xf, BANKS, false); }
DI float add32(float a, float b) { auto s = __builtin_amdgcn_permlane32_swap(fu(a), fu(b), false, false); return uf(s[0]) + uf(s[1]); }
DI float add16(float a, float b) { auto s = __builtin_amdgcn_permlane16_swap(fu(a), fu(b), false, false); return uf(s[0]) + uf(s[1]); }
DI float max32(float a) { auto s = __builtin_amdgcn_permlane32_swap(fu(a), fu(a), false, false); return fmx(uf(s[0]), uf(s[1])); }
DI float max16(float a) { auto s = __builtin_amdgcn_permlane16_swap(fu(a), fu(a), false, false); return fmx(uf(s[0]), uf(s[1])); }
DI float wsum(float v) {
  v += ror<8>(v); v += ror<4>(v); v += ror<2>(v); v += ror<1>(v);
  v = add16(v, v);
  return add32(v, v);
}
DI f32x4 sel(bool c, f32x4 a, f32x4 b) { return f32x4{c ? a.x : b.x, c ? a.y : b.y, c ? a.z : b.z, c ? a.w : b.w}; }
DI void lds_signal(u32 a) {
  asm volatile("s_waitcnt lgkmcnt(0)" ::: "memory");
  lw1(a, 1u);
}
DI void lds_wait4(u32 a, u32& spins) {
  for (;;) {
    asm volatile("" ::: "memory");
    const u32x4 f = lr4(a);
    if (__builtin_amdgcn_readfirstlane(f.x & f.y & f.z & f.w) || ++spins > SPIN_MAX) break;
  }
  asm volatile("" ::: "memory");
}
DI bool none(bool bad) { return __builtin_amdgcn_ballot_w64(bad) == 0; }
DI float ex2(float v) { return __builtin_amdgcn_exp2f(v); }
DI float rcp(float v) { return __builtin_amdgcn_rcpf(v); }
template <int N> DI void nap() { if (N > 0) __builtin_amdgcn_s_sleep(N); }

DI void red8(const float (&a)[8], float& o0, float& o1) {
  float r0 = add32(a[0], a[4]), r1 = add32(a[1], a[5]), r2 = add32(a[2], a[6]), r3 = add32(a[3], a[7]);
  float s0 = add16(r0, r2), s1 = add16(r1, r3);
  o0 = s0 + ror<8>(s0);
  o1 = s1 + ror<8>(s1);
}
DI float red4(f32x4 a) {
  float r0 = add32(a.x, a.z), r1 = add32(a.y, a.w);
  float s = add16(r0, r1);
  return s + ror<8>(s);
}

#define STAMP(i)
#define STAMPW(i)

__global__ __launch_bounds__(512, 2) void mla_ffn_decode(Args a) {
  const u32 tid = threadIdx.x, bid = blockIdx.x, lane = tid & 63;
  const u32 w = __builtin_amdgcn_readfirstlane(tid >> 6);
  STAMP(0);
  const u32 xcd = bid & 7, r = bid >> 3, grp = r & 1, j = (r >> 1) & 7, h = grp * 16 + xcd * 2 + (r >> 4);
  const u32 ab = xcd >> 1, aks = (xcd & 1) * 16 + (r >> 1), hg = 1 - grp, sp = ab * 32 + aks;
  const u32 mq = j >> 1, dh = j & 1;
  const u32 l15 = lane & 15, g = lane >> 4, bq = lane & 3, u = (lane >> 2) & 3, l7 = lane & 7, l3 = lane >> 3;
  const u32 tq = u, tp = bq;
  const bool ffn = w == 3 && h < 28, fin = w == 7 && h >= 24;
  if (w == 0) lw2(L_FL + lane * 8, u32x2{0, 0});
  BARRIER();
  const char* ws = a.ws;
  const u32 tag = *(const __attribute__((address_space(4))) u32*)ws + 1;
  u32 spins = 0;

  const u32 f1 = (l3 >> 1) & 1;
  const u32 trn = (g * 8 + tq) * 128 + (tp & 1) * 8, trf = ((tq >> 1) | ((g & 1) << 1)) << 1;
  const u32x4 sent4 = {SENT, SENT, SENT, SENT};
  u32 o5, ohf, ost, o3, o3b, bfr, va, frag[4];
  u32x4 xf0, xf1;
#define COMMON_ADDR()                                                                                        \
  do {                                                                                                       \
    o5 = (j * 8 + bq * 2 + (u >> 1)) * 4096 + h * 128 + (u & 1) * 64 + g * 16;                               \
    ohf = W_HF + h * 8192 + j * 1024 + lane * 16;                                                            \
    ost = W_ST + (hg * 16 + l15) * 2048 + sp * 16;                                                           \
    o3 = W_H3 + (((hg * 16 + l7) * 8 + w) * 128 + sp) * 128 + (((lane >> 3) & 1) * 4 + g) * 16;              \
    o3b = o3 + 8 * 8 * 16384;                                                                                \
    bfr = g * 64 + bq * 16;                                                                                  \
    va = (g * 4 + tq) * 1024 + (((w * 8 + (tp >> 1)) ^ (((g & 1) * 4 + tq) << 1)) << 4) + (tp & 1) * 8;      \
    _Pragma("unroll") for (int i = 0; i < 4; ++i) frag[i] = (l15 * 1024 + ((g ^ (l7 << 1)) << 4)) ^ (i << 6); \
    PIN(o5); PIN(ohf); PIN(ost); PIN(o3); PIN(o3b); PIN(bfr); PIN(va);                                       \
    PIN(frag[0]); PIN(frag[1]); PIN(frag[2]); PIN(frag[3]);                                                  \
  } while (0)
  const char* kvb = a.ckv + (size_t)(ab * 4096 + aks * 128 + (w & 3) * 32) * 1024;

  if (w < 4) {
    u32x4 xv;
    ldg4(xv, a.x, w * 1024 + lane * 16);
    const u32 vq = ((mq * 128 + w * 32 + l3) * 32 + h) * 256 + dh * 128;
    const u32 vq0 = vq + ((l7 ^ (f1 << 1)) << 4), vq1 = vq + 8 * 8192 + ((l7 ^ ((f1 | 2) << 1)) << 4);
    dma<1>(a.wq, vq0, L_X + w * 4096);
    dma<1>(a.wq, vq1, L_X + w * 4096 + 1024);
    dma<1>(a.wq + 16 * 8192, vq0, L_X + w * 4096 + 2048);
    dma<1>(a.wq + 16 * 8192, vq1, L_X + w * 4096 + 3072);
#define UK_DMA()                                                                                              \
  _Pragma("unroll") for (int i = 0; i < 4; ++i)                                                               \
      dma<1>(a.wuk + i * 4 * 8192, ((j * 64 + w * 16 + g) * 32 + h) * 256 + ((l15 ^ (i * 4 + g)) << 4), L_Y + w * 4096 + i * 1024)
    if (!P_UKL) { UK_DMA(); }
    COMMON_ADDR();
    u32 oc = w == 0 ? ohf : w == 1 ? W_H4 + h * 8192 + l3 * 1024 + j * 128 + l7 * 16 : w == 2 ? W_H5 + o5 : W_HY + o5;
    u32 taq = L_X + trn + (((2 * w + (tp >> 1)) ^ trf) << 4);
    u32 o1s = W_H1 + h * 16384 + dh * 8192 + w * 2048 + mq * 512 + (g * 8 + l7) * 16;
    u32 o1 = W_H1 + h * 16384 + w * 4096 + lane * 16;
    u32 iq = L_Q0 + (4 * w + (lane >> 5) * 2 + (g & 1)) * 64 + bq * 16 + (l3 & 1) * 8 + (u & 1) * 4;
    u32 ua = L_Y + (w * 32 + l15) * 256 + ((g ^ l15) << 4);
    u32 o2s = W_H2 + (bq * 32 + h) * 2048 + j * 256 + w * 128 + u * 64 + g * 16;
    u32 o2 = W_H2 + (ab * 32 + hg * 16 + 4 * w) * 2048 + lane * 16, o2b = o2 + 4096;
    u32 i0 = L_X + 4 * w * 1024 + (((lane >> 1) ^ (((4 * w) & 7) << 1)) << 4) + (lane & 1) * 8;
    PIN(oc); PIN(taq); PIN(o1s); PIN(o1); PIN(iq); PIN(ua); PIN(o2s); PIN(o2); PIN(o2b); PIN(i0);

    WAITV(P_UKL ? 4 : 8);
    STAMP(19);
    PIN(xv);
    {
      float v[8] = {lo(xv.x), hi(xv.x), lo(xv.y), hi(xv.y), lo(xv.z), hi(xv.z), lo(xv.w), hi(xv.w)};
      const float mean = wsum(((v[0] + v[1]) + (v[2] + v[3])) + ((v[4] + v[5]) + (v[6] + v[7]))) * (1.f / 512.f);
      float d[8];
#pragma unroll
      for (int i = 0; i < 8; ++i) { d[i] = v[i] - mean; d[i] *= d[i]; }
      const float var = rbf(wsum(((d[0] + d[1]) + (d[2] + d[3])) + ((d[4] + d[5]) + (d[6] + d[7]))) * (1.f / 512.f)), mb = rbf(mean);
      const float inv = rcp(rbf(__builtin_amdgcn_sqrtf(rbf(var + 1e-5f))));
      u32x4 o;
#pragma unroll
      for (int i = 0; i < 4; ++i) { const u32 c = pk(v[2 * i] - mb, v[2 * i + 1] - mb); o[i] = pk(lo(c) * inv, hi(c) * inv); }
      PIN(o);
      if (g == mq) lw4(L_XN + l15 * 64 + w * 16, o);
    }
    WAITV(P_UKL ? 0 : 4);
    STAMP(1);
    lds_signal(L_FL + w * 4);
    if (P_UKL == 1) { UK_DMA(); }
    lds_wait4(L_FL, spins);
    {
      u32x4 fa[4], fb[4];
#pragma unroll
      for (int ks = 0; ks < 4; ++ks) { fa[ks] = ltr(taq + ks * 4096, taq + ks * 4096 + 512); fb[ks] = lr4(L_XN + ks * 256 + bfr); }
      FENCE();
      f32x4 c0 = mfma(fa[0], fb[0], f32x4{0, 0, 0, 0}), c1 = mfma(fa[1], fb[1], f32x4{0, 0, 0, 0});
      c0 = mfma(fa[2], fb[2], c0);
      c1 = mfma(fa[3], fb[3], c1);
      xf0 = dh ? fb[2] : fb[0];
      xf1 = dh ? fb[3] : fb[1];
      const f32x4 acc = c0 + c1;
      u32x4 o = {fu((u & 1) ? acc.z : acc.x), tag, fu((u & 1) ? acc.w : acc.y), tag};
      PIN(o);
      if (u < 2) stc4(ws, o1s, o);
    }
    STAMP(2);
    if (P_ST == 1) lw1(L_FL + 32 + w * 4, 1u);
    if (P_UKL == 2) { UK_DMA(); }
    stc4(ws, oc, sent4);
    {
      u32x4 p0, p1, p2, p3;
      nap<P_D1>();
      for (;;) {
        ldc4<0>(p0, ws, o1); ldc4<1024>(p1, ws, o1); ldc4<2048>(p2, ws, o1); ldc4<3072>(p3, ws, o1);
        if (P_ST == 2) lw1(L_FL + 32 + w * 4, 1u);
        WAITV(0);
        PIN(p0); PIN(p1); PIN(p2); PIN(p3);
        const u32 bad = (p0.y ^ tag) | (p0.w ^ tag) | (p1.y ^ tag) | (p1.w ^ tag) | (p2.y ^ tag) | (p2.w ^ tag) | (p3.y ^ tag) | (p3.w ^ tag);
        if (none(bad != 0) || ++spins > SPIN_MAX) break;
      }
      lw1(iq, pk(add32(uf(p0.x) + uf(p1.x), uf(p2.x) + uf(p3.x)), add32(uf(p0.z) + uf(p1.z), uf(p2.z) + uf(p3.z))));
    }
    STAMP(3);
    if (P_ST == 3) lw1(L_FL + 32 + w * 4, 1u);
    lds_signal(L_FL + 16 + w * 4);
    if (w < 2) {
      u32x4 fb[4], f0[4], f1v[4];
      lds_wait4(L_FL + 16, spins);
#pragma unroll
      for (int ks = 0; ks < 4; ++ks) { fb[ks] = lr4(L_Q0 + bfr + ks * 256); f0[ks] = lr4(ua ^ (ks << 6)); f1v[ks] = lr4((ua ^ (ks << 6)) + 4096); }
      FENCE();
      f32x4 c0 = {0, 0, 0, 0}, c1 = {0, 0, 0, 0};
#pragma unroll
      for (int ks = 0; ks < 4; ++ks) { c0 = mfma(f0[ks], fb[ks], c0); c1 = mfma(f1v[ks], fb[ks], c1); }
      const float sc = 0.08838834764831845f * 1.4426950408889634f;
      const f32x4 c = sel(u & 1, c1, c0) * sc;
      u32x4 o = {pk(c.x, c.y), tag, pk(c.z, c.w), tag};
      PIN(o);
      if (u < 2) stc4(ws, o2s, o);
      STAMP(4);
      nap<P_D2>();
    } else {
      lds_wait4(L_FL + 16, spins);
      nap<P_D2 + 6>();
    }
    u32x4 s[8];
    for (;;) {
      ldc4<0>(s[0], ws, o2); ldc4<1024>(s[1], ws, o2); ldc4<2048>(s[2], ws, o2); ldc4<3072>(s[3], ws, o2);
      ldc4<0>(s[4], ws, o2b); ldc4<1024>(s[5], ws, o2b); ldc4<2048>(s[6], ws, o2b); ldc4<3072>(s[7], ws, o2b);
      WAITV(0);
      u32 bad = 0;
#pragma unroll
      for (int i = 0; i < 8; ++i) { PIN(s[i]); bad |= (s[i].y ^ tag) | (s[i].w ^ tag); }
      if (none(bad != 0) || ++spins > SPIN_MAX) break;
    }
#pragma unroll
    for (int i = 0; i < 8; ++i) lw2((i0 ^ ((i >> 1) << 5)) + (i >> 1) * 1024 + (i & 1) * 512, u32x2{s[i].x, s[i].z});
    STAMP(5);
    BARRIER();
  } else {
    COMMON_ADDR();
    if (P_ST) lds_wait4(L_FL + 32, spins);
    nap<P_DS>();
    STAMP(2);
    const u32 lb = (w & 3) * 32768, off = grp ? P_OFF : 0;
#pragma unroll
    for (int i = 0; i < 31; ++i) {
      if (i >= P_W) WAITV(P_W - 1);
      const u32 row = i < 31 - P_OFF ? i + off : i - (31 - P_OFF) + (grp ? 0 : 31 - P_OFF);
      dma<KVNT>(kvb + row * 1024, (lane ^ ((row & 7) << 1)) << 4, lb + row * 1024);
    }
    STAMP(4);
    BARRIER();
    dma<KVNT>(kvb + 31 * 1024, (lane ^ 14) << 4, lb + 31 * 1024);
  }
  stc4(ws, o3, sent4);
  stc4(ws, o3b, sent4);
  const u32 lkv = w * 16384;
  const u32 vuk = ((j * 64 + w * 8 + g) * 32 + h) * 256;
  dma<1>(a.wuv, vuk + ((l15 ^ (g << 1)) << 4), L_Y + w * 2048);
  dma<1>(a.wuv, vuk + 4 * 8192 + ((l15 ^ ((4 + g) << 1)) << 4), L_Y + w * 2048 + 1024);

  u32x4 fr[16];
#pragma unroll
  for (int ks = 0; ks < 16; ++ks) fr[ks] = lr4(L_X + frag[ks & 3] + (ks >> 2) * 256);
#pragma unroll
  for (int i = 0; i < 4; ++i) { frag[i] += lkv; PIN(frag[i]); }
  STAMP(6);
  BARRIER();
  if (ffn) {
#pragma unroll
    for (int i = 0; i < 8; ++i)
      dma<1>(a.w1 + i * 8 * 3584, (j * 64 + l3) * 3584 + h * 128 + ((l7 ^ ((f1 | ((i & 1) << 1)) << 1)) << 4), L_W1 + i * 1024);
  }
  u32 imx = L_MX + l15 * 32, ip = L_P + lane * 16;
  PIN(imx); PIN(ip);
  if (w >= 4) WAITV(4);
  STAMP(7);
  BARRIER();

  f32x4 sv;
  {
    u32x4 fa[16];
#pragma unroll
    for (int ks = 0; ks < 16; ++ks) fa[ks] = lr4(frag[ks & 3] + (ks >> 2) * 256);
    FENCE();
    f32x4 c0 = {0, 0, 0, 0}, c1 = {0, 0, 0, 0};
#pragma unroll
    for (int ks = 0; ks < 16; ks += 2) { c0 = mfma(fa[ks], fr[ks], c0); c1 = mfma(fa[ks + 1], fr[ks + 1], c1); }
    sv = c0 + c1;
    float m = max32(max16(fmx(fmx(sv.x, sv.y), fmx(sv.z, sv.w))));
    PIN(m);
    if (g == 0) lw1(imx + w * 4, fu(m));
  }
  STAMP(8);
  BARRIER();
  float mx;
  {
    const u32x4 m0 = lr4(imx), m1 = lr4(imx + 16);
    FENCE();
#pragma unroll
    for (int ks = 0; ks < 4; ++ks)
#pragma unroll
      for (int t = 0; t < 4; ++t) fr[t * 4 + ks] = ltr((va ^ (t << 5)) + ks * 32768, (va ^ (t << 5)) + ks * 32768 + 16384);
    FENCE();
    mx = fmx(fmx(fmx(uf(m0.x), uf(m0.y)), fmx(uf(m0.z), uf(m0.w))), fmx(fmx(uf(m1.x), uf(m1.y)), fmx(uf(m1.z), uf(m1.w))));
    const u32 p0 = pk(ex2(sv.x - mx), ex2(sv.y - mx)), p1 = pk(ex2(sv.z - mx), ex2(sv.w - mx));
    lw2(ip + (w >> 1) * 1024 + (w & 1) * 8, u32x2{p0, p1});
    float l = (lo(p0) + hi(p0)) + (lo(p1) + hi(p1));
    l = add16(l, l);
    l = add32(l, l);
    PIN(l);
    if (g == 0) lw1(imx + (L_LS - L_MX) + w * 4, fu(l));
    if (ffn) WAITV(10); else WAITV(2);
  }
  STAMP(9);
  BARRIER();
#define WO_DMA()                                                                          \
  do {                                                                                    \
    if (ffn) {                                                                            \
      _Pragma("unroll") for (int i = 0; i < 8; ++i)                                       \
          dma<1>(a.w2 + i * 8192, (h * 64 + l3) * 1024 + j * 128 + ((l7 ^ (l3 & 6)) << 4), L_W2 + i * 1024); \
    }                                                                                     \
    const u32 vo = (h * 128 + w * 16 + l3) * 1024 + j * 128;                              \
    dma<1>(a.wo, vo + ((l7 ^ (f1 << 1)) << 4), L_WO + w * 2048);                          \
    dma<1>(a.wo, vo + 8 * 1024 + ((l7 ^ ((f1 | 2) << 1)) << 4), L_WO + w * 2048 + 1024);  \
  } while (0)
  {
    u32x4 pf[4];
#pragma unroll
    for (int ks = 0; ks < 4; ++ks) pf[ks] = lr4(ip + ks * 1024);
    if (w == 7) {
      const u32x4 l0 = lr4(imx + (L_LS - L_MX)), l1 = lr4(imx + (L_LS - L_MX) + 16);
      u32x4 o = {fu(mx), tag, fu(((uf(l0.x) + uf(l0.y)) + (uf(l0.z) + uf(l0.w))) + ((uf(l1.x) + uf(l1.y)) + (uf(l1.z) + uf(l1.w)))), tag};
      PIN(o);
      if (lane < 16) stc4(ws, ost, o);
    }
    FENCE();
    f32x4 o[4] = {{0, 0, 0, 0}, {0, 0, 0, 0}, {0, 0, 0, 0}, {0, 0, 0, 0}};
#pragma unroll
    for (int ks = 0; ks < 4; ++ks)
#pragma unroll
      for (int t = 0; t < 4; ++t) o[t] = mfma(fr[t * 4 + ks], pf[ks], o[t]);
    const u32x4 r0 = {pk(o[0].x, o[0].y), pk(o[0].z, o[0].w), pk(o[1].x, o[1].y), pk(o[1].z, o[1].w)};
    const u32x4 r1 = {pk(o[2].x, o[2].y), pk(o[2].z, o[2].w), pk(o[3].x, o[3].y), pk(o[3].z, o[3].w)};
    u32x4 n0, n1;
#pragma unroll
    for (int e = 0; e < 4; ++e) { n0[e] = canon(ror8m<0xC>(r0[e], r1[e])); n1[e] = canon(ror8m<0x3>(r1[e], r0[e])); }
    stc4(ws, o3, n0);
    stc4(ws, o3b, n1);
  }
  STAMP(10);
  if (P_WO == 0) WO_DMA();

  const u32 fb = (h >> 1) & 3, fm = j * 64 + (h & 1) * 32 + l7 * 4 + (lane >> 5) * 2 + ((lane >> 4) & 1);
  u32 osn = W_ST + h * 2048 + ((w >> 1) * 32 + l3) * 16 + (w & 1) * 256, oso = osn ^ 256;
  u32 od = W_H3 + ((h * 8 + j) * 128 + w * 16) * 128 + lane * 16;
  u32 icb = L_CB + l7 * 128 + ((((w & 1) * 4 + (w >> 1)) ^ l7) << 4) + (lane >> 5) * 8 + ((lane >> 4) & 1) * 4;
  u32 vu = L_Y + (g * 4 + tq) * 256 + (((2 * w + (tp >> 1)) ^ (((g & 1) * 4 + tq) << 1)) << 4) + (tp & 1) * 8;
  u32 rcb = L_CB + g * 128 + ((l7 ^ g) << 4);
  u32 o4s = W_H4 + h * 8192 + w * 1024 + j * 128 + bq * 32 + g * 8;
  PIN(osn); PIN(oso); PIN(od); PIN(icb); PIN(vu); PIN(rcb); PIN(o4s);
  if (ffn) {
    WAITV(P_WO == 0 ? 12 : 2);
    u32x4 fa[8];
#pragma unroll
    for (int t = 0; t < 4; ++t) {
      const u32 ta = L_W1 + trn + (((2 * t + (tp >> 1)) ^ trf) << 4);
      fa[2 * t] = ltr(ta, ta + 512);
      fa[2 * t + 1] = ltr(ta + 4096, ta + 4096 + 512);
    }
    FENCE();
    f32x4 d[4];
#pragma unroll
    for (int t = 0; t < 4; ++t) d[t] = mfma(fa[2 * t], xf0, f32x4{0, 0, 0, 0});
#pragma unroll
    for (int t = 0; t < 4; ++t) d[t] = mfma(fa[2 * t + 1], xf1, d[t]);
    stc4(ws, ohf, canon4(sel(u & 2, sel(u & 1, d[3], d[2]), sel(u & 1, d[1], d[0]))));
    nap<(P_D3 > 10 ? P_D3 - 10 : 0)>();
  } else {
    nap<P_D3>();
  }
  {
    u32x4 e0, e1, e2, e3, d0, d1;
    STAMP(11);
    for (;;) {
      ldc4<0>(e0, ws, osn); ldc4<128>(e1, ws, osn); ldc4<0>(e2, ws, oso); ldc4<128>(e3, ws, oso);
      WAITV(0);
      PIN(e0); PIN(e1); PIN(e2); PIN(e3);
      const u32 bad = (e0.y ^ tag) | (e0.w ^ tag) | (e1.y ^ tag) | (e1.w ^ tag) | (e2.y ^ tag) | (e2.w ^ tag) | (e3.y ^ tag) | (e3.w ^ tag);
      if (none(bad != 0) || ++spins > SPIN_MAX) break;
    }
    STAMP(17);
    nap<P_D3B>();
    ldc4<0>(d0, ws, od); ldc4<1024>(d1, ws, od);
    if (P_WO == 1) WO_DMA();
    float m = fmx(fmx(uf(e0.x), uf(e1.x)), fmx(uf(e2.x), uf(e3.x)));
    m = max32(max16(fmx(m, ror<8>(m))));
    float w0 = ex2(uf(e0.x) - m), w1 = ex2(uf(e1.x) - m);
    float l = w0 * uf(e0.z) + w1 * uf(e1.z) + ex2(uf(e2.x) - m) * uf(e2.z) + ex2(uf(e3.x) - m) * uf(e3.z);
    l += ror<8>(l);
    l = add16(l, l);
    l = add32(l, l);
    const float il = rcp(l);
    w0 *= il; w1 *= il;
    PIN(w0); PIN(w1);
    if (P_WO != 1) WAITV(0); else if (ffn) WAITV(10); else WAITV(2);
    for (;;) {
      PIN(d0); PIN(d1);
      if (none(umax(umax4(d0), umax4(d1)) == SENT) || ++spins > SPIN_MAX) break;
      ldc4<0>(d0, ws, od); ldc4<1024>(d1, ws, od);
      WAITV(0);
    }
    STAMP(12);
    if (P_WO == 2) WO_DMA();
    float acc[8];
#pragma unroll
    for (int e = 0; e < 4; ++e) {
      acc[2 * e] = w0 * lo(d0[e]) + w1 * lo(d1[e]);
      acc[2 * e + 1] = w0 * hi(d0[e]) + w1 * hi(d1[e]);
    }
    float c0, c1;
    red8(acc, c0, c1);
    u32 o = pk(c0, c1);
    PIN(o);
    if (!(lane & 8)) lw1(icb, o);
  }
  STAMP(13);
  BARRIER();
  {
    const u32x4 a0 = ltr(vu, vu + 4096), b0 = lr4(rcb), a1 = ltr(vu + 8192, vu + 8192 + 4096), b1 = lr4((rcb ^ 64) + 512);
    FENCE();
    const f32x4 c = mfma(a0, b0, f32x4{0, 0, 0, 0}) + mfma(a1, b1, f32x4{0, 0, 0, 0});
    u32x2 v = {canon(pk(c.x + ror<4>(c.x), c.y + ror<4>(c.y))), canon(pk(c.z + ror<4>(c.z), c.w + ror<4>(c.w)))};
    PIN(v);
    if (u == 0) stc2(ws, o4s, v);
  }
  STAMP(14);
  if (P_WO != 0) { WAITV(1); BARRIER(); }
  if (w < 2) {
    const u32 t0 = L_WO + trn + (((4 * w + (tp >> 1)) ^ trf) << 4), t1 = t0 ^ 32;
    u32 o4 = W_H4 + h * 8192 + (g >> 1) * 1024 + u * 128 + bq * 32 + (g & 1) * 16, o4b = o4 + 4096;
    u32 o5s = W_H5 + (j * 8 + bq * 2 + w) * 4096 + h * 128 + u * 64 + g * 16;
    u32x4 fa[8];
#pragma unroll
    for (int ks = 0; ks < 4; ++ks) { fa[ks] = ltr(t0 + ks * 4096, t0 + ks * 4096 + 512); fa[4 + ks] = ltr(t1 + ks * 4096, t1 + ks * 4096 + 512); }
#pragma unroll
    for (int i = 0; i < 8; ++i) PIN(fa[i]);
    PIN(o4); PIN(o4b); PIN(o5s);
    nap<P_D4>();
    u32x4 p[8];
    for (;;) {
      ldc4<0>(p[0], ws, o4); ldc4<512>(p[1], ws, o4); ldc4<2048>(p[2], ws, o4); ldc4<2560>(p[3], ws, o4);
      ldc4<0>(p[4], ws, o4b); ldc4<512>(p[5], ws, o4b); ldc4<2048>(p[6], ws, o4b); ldc4<2560>(p[7], ws, o4b);
      WAITV(0);
      u32 t = 0;
#pragma unroll
      for (int i = 0; i < 8; ++i) { PIN(p[i]); t = umax(t, umax4(p[i])); }
      if (none(t == SENT) || ++spins > SPIN_MAX) break;
    }
    STAMP(15);
    f32x4 c[4] = {{0, 0, 0, 0}, {0, 0, 0, 0}, {0, 0, 0, 0}, {0, 0, 0, 0}};
#pragma unroll
    for (int ks = 0; ks < 4; ++ks) {
      c[0] = mfma(fa[ks], p[2 * ks], c[0]);
      c[1] = mfma(fa[4 + ks], p[2 * ks], c[1]);
      c[2] = mfma(fa[ks], p[2 * ks + 1], c[2]);
      c[3] = mfma(fa[4 + ks], p[2 * ks + 1], c[3]);
    }
    f32x4 c0 = c[0] + c[2], c1 = c[1] + c[3];
#pragma unroll
    for (int e = 0; e < 4; ++e) {
      c0[e] += ror<8>(c0[e]); c1[e] += ror<8>(c1[e]);
      c0[e] += ror<4>(c0[e]); c1[e] += ror<4>(c1[e]);
    }
    u32x4 o = canon4(sel(u & 1, c1, c0));
    PIN(o);
    if (u < 2) stc4(ws, o5s, o);
  } else if (ffn) {
    const u32 of = W_HF + h * 8192 + lane * 16;
    const u32 f2 = (((g & 1) << 1) | (tq >> 1)) << 1;
    u32x4 p[8], fa[8];
    u32x2 b1v;
    ldg2(b1v, a.b1, (h * 64 + u * 16 + g * 4) * 2);
    for (;;) {
      ldc4<0>(p[0], ws, of); ldc4<1024>(p[1], ws, of); ldc4<2048>(p[2], ws, of); ldc4<3072>(p[3], ws, of);
      ldc4<0>(p[4], ws, of + 4096); ldc4<1024>(p[5], ws, of + 4096); ldc4<2048>(p[6], ws, of + 4096); ldc4<3072>(p[7], ws, of + 4096);
#pragma unroll
      for (int t = 0; t < 4; ++t) {
        const u32 ta = L_W2 + (g * 4 + tq) * 128 + (((2 * t + (tp >> 1)) ^ f2) << 4) + (tp & 1) * 8;
        fa[2 * t] = ltr(ta, ta + 2048);
        fa[2 * t + 1] = ltr(ta + 4096, ta + 4096 + 2048);
      }
      WAITV(0);
      u32 t = 0;
#pragma unroll
      for (int i = 0; i < 8; ++i) { PIN(p[i]); t = umax(t, umax4(p[i])); }
      if (none(t == SENT) || ++spins > SPIN_MAX) break;
    }
    PIN(b1v);
    const float bb[4] = {lo(b1v.x), hi(b1v.x), lo(b1v.y), hi(b1v.y)};
    float ge[4];
#pragma unroll
    for (int e = 0; e < 4; ++e) {
      const float s = ((uf(p[0][e]) + uf(p[1][e])) + (uf(p[2][e]) + uf(p[3][e]))) + ((uf(p[4][e]) + uf(p[5][e])) + (uf(p[6][e]) + uf(p[7][e])));
      const float t = rbf(rbf(s) + bb[e]);
      const float z = ex2(2.302208198f * (t + 0.044715f * t * t * t));
      ge[e] = t - t * rcp(z + 1.f);
    }
    lw2(L_HB + (u >> 1) * 256 + bfr + (u & 1) * 8, u32x2{pk(ge[0], ge[1]), pk(ge[2], ge[3])});
    asm volatile("s_waitcnt lgkmcnt(0)" ::: "memory");
    const u32x4 hb0 = lr4(L_HB + bfr), hb1 = lr4(L_HB + 256 + bfr);
    f32x4 d[4];
#pragma unroll
    for (int t = 0; t < 4; ++t) d[t] = mfma(fa[2 * t], hb0, f32x4{0, 0, 0, 0});
#pragma unroll
    for (int t = 0; t < 4; ++t) d[t] = mfma(fa[2 * t + 1], hb1, d[t]);
    stc4(ws, W_HY + o5, canon4(sel(u & 2, sel(u & 1, d[3], d[2]), sel(u & 1, d[1], d[0]))));
    STAMP(15);
  } else if (fin) {
    u32 fy = W_HY + (j * 8 + (h & 7)) * 4096 + lane * 16, fy3 = lane < 32 ? fy + 3072 : fy, fs = fy + (W_H5 - W_HY);
    u32 oo = (fb * 512 + fm) * 2;
    u32 xo, b2o;
    ldgh(xo, a.x, oo);
    ldgh(b2o, a.b2, fm * 2);
    PIN(fy); PIN(fy3); PIN(fs); PIN(oo);
    nap<P_D5>();
    u32x4 p0, p1, p2, p3;
    for (;;) {
      ldc4<0>(p0, ws, fy); ldc4<1024>(p1, ws, fy); ldc4<2048>(p2, ws, fy); ldc4<0>(p3, ws, fy3);
      WAITV(0);
      PIN(p0); PIN(p1); PIN(p2); PIN(p3);
      if (none(umax(umax(umax4(p0), umax4(p1)), umax(umax4(p2), umax4(p3))) == SENT) || ++spins > SPIN_MAX) break;
    }
    STAMP(15);
    u32x4 q0, q1, q2, q3;
    ldc4<0>(q0, ws, fs); ldc4<1024>(q1, ws, fs); ldc4<2048>(q2, ws, fs); ldc4<3072>(q3, ws, fs);
    PIN(xo); PIN(b2o);
    const f32x4 z = {0, 0, 0, 0};
    const f32x4 sy = __builtin_bit_cast(f32x4, p0) + __builtin_bit_cast(f32x4, p1) + __builtin_bit_cast(f32x4, p2) + sel(lane < 32, __builtin_bit_cast(f32x4, p3), z);
    const float y = rbf(rbf(red4(sy)) + lo(b2o));
    float gate = rbf(y * rcp(1.f + ex2(-1.4426950408889634f * y)));
    PIN(gate);
    WAITV(0);
    for (;;) {
      PIN(q0); PIN(q1); PIN(q2); PIN(q3);
      if (none(umax(umax(umax4(q0), umax4(q1)), umax(umax4(q2), umax4(q3))) == SENT) || ++spins > SPIN_MAX) break;
      ldc4<0>(q0, ws, fs); ldc4<1024>(q1, ws, fs); ldc4<2048>(q2, ws, fs); ldc4<3072>(q3, ws, fs);
      WAITV(0);
    }
    const f32x4 s = (__builtin_bit_cast(f32x4, q0) + __builtin_bit_cast(f32x4, q1)) + (__builtin_bit_cast(f32x4, q2) + __builtin_bit_cast(f32x4, q3));
    const float o = lo(xo) + rbf(rbf(red4(s)) * gate);
    if (!(lane & 8)) *(u16*)(a.out + oo) = (u16)pk(o, o);
    if (h == 24 && j == 0 && lane == 0) stc1(ws, 0, tag);
  }
  STAMP(18);
}

__global__ __launch_bounds__(512, 2) void probe(char* ws, const char* kv) {
}
extern "C" void kernel_launch(void* const* d_in, const int* in_sizes, int n_in, void* d_out, int out_size, void* d_ws, size_t ws_size, hipStream_t stream) {
  Args a;
  a.x = (const char*)d_in[0];
  a.ckv = (const char*)d_in[1];
  a.wq = (const char*)d_in[2];
  a.wuk = (const char*)d_in[3];
  a.wuv = (const char*)d_in[4];
  a.wo = (const char*)d_in[5];
  a.w1 = (const char*)d_in[7];
  a.b1 = (const char*)d_in[8];
  a.w2 = (const char*)d_in[9];
  a.b2 = (const char*)d_in[10];
  a.out = (char*)d_out;
  a.ws = (char*)d_ws;
  (void)hipFuncSetAttribute((const void*)mla_ffn_decode, hipFuncAttributeMaxDynamicSharedMemorySize, L_TOTAL);
  mla_ffn_decode<<<256, 512, L_TOTAL, stream>>>(a);
  (void)hipFuncSetAttribute((const void*)probe, hipFuncAttributeMaxDynamicSharedMemorySize, L_TOTAL);
  probe<<<256, 512, PRB == 1 ? L_TOTAL : 0, stream>>>(a.ws, a.ckv);
}
```

```cpp
#define AMP 1
#include <hip/hip_runtime.h>
#include <hip/hip_bf16.h>
#include <stdint.h>

typedef unsigned u32;
typedef unsigned short u16;
typedef u32 u32x2 __attribute__((ext_vector_type(2)));
typedef u32 u32x4 __attribute__((ext_vector_type(4)));
typedef float f32x2 __attribute__((ext_vector_type(2)));
typedef float f32x4 __attribute__((ext_vector_type(4)));
typedef __bf16 bf16x2 __attribute__((ext_vector_type(2)));
typedef __bf16 bf16x8 __attribute__((ext_vector_type(8)));
typedef short s16x4 __attribute__((ext_vector_type(4)));
#define LAS __attribute__((address_space(3)))
#define DI __device__ __forceinline__

#define P_W 6
#define P_ST 1
#define P_OFF 0
#define P_DS 0
#define KVNT 0
#define P_D1 12
#define P_D2 14
#define P_D3 8
#define P_D4 12
#define P_D5 40
#define P_WO 1
#define P_UKL 1
#define P_D3B 0
constexpr u32 SPIN_MAX = 1u << 16;
constexpr u32 SENT = 0xFFFFFFFFu;

constexpr u32 W_H1 = 0x1000, W_H2 = W_H1 + 0x80000, W_ST = W_H2 + 0x40000, W_HF = W_ST + 0x10000;
constexpr u32 W_H4 = W_HF + 0x40000, W_H5 = W_H4 + 0x40000, W_HY = W_H5 + 0x40000, W_H3 = W_HY + 0x40000;
constexpr u32 W_DG = W_H3 + 0x400000;

constexpr u32 L_X = 131072, L_Y = L_X + 16384, L_TOTAL = L_Y + 16384;
constexpr u32 L_Q0 = 31 * 1024, L_XN = 63 * 1024, L_FL = 95 * 1024;
constexpr u32 L_W1 = L_X, L_MX = L_X + 8192, L_LS = L_X + 8704, L_P = L_X + 9216, L_CB = L_X + 13312, L_CX = L_X + 14336, L_HB = L_X + 15360;
constexpr u32 L_WO = 0, L_W2 = 16384;

struct Args {
  const char *x, *ckv, *wq, *wuk, *wuv, *wo, *w1, *b1, *w2, *b2;
  char *out, *ws;
};

#define WAITV(n) asm volatile("s_waitcnt vmcnt(%0)" ::"i"(n) : "memory")
#define BARRIER() asm volatile("s_waitcnt lgkmcnt(0)\n\ts_barrier" ::: "memory")
#define PIN(v) asm volatile("" : "+v"(v))
#define FENCE() __builtin_amdgcn_sched_barrier(0)

template <int NT> DI void dma(const char* base, u32 voff, u32 lds) {
  u32 keep;
  lds = __builtin_amdgcn_readfirstlane(lds);
  if (NT)
    asm volatile("s_mov_b32 %0, m0\n\ts_mov_b32 m0, %3\n\ts_nop 0\n\tglobal_load_lds_dwordx4 %1, %2 nt\n\ts_mov_b32 m0, %0"
                 : "=&s"(keep) : "v"(voff), "s"(base), "s"(lds) : "memory");
  else
    asm volatile("s_mov_b32 %0, m0\n\ts_mov_b32 m0, %3\n\ts_nop 0\n\tglobal_load_lds_dwordx4 %1, %2\n\ts_mov_b32 m0, %0"
                 : "=&s"(keep) : "v"(voff), "s"(base), "s"(lds) : "memory");
}
template <int OFF> DI void ldc4(u32x4& d, const char* base, u32 voff) {
  asm volatile("global_load_dwordx4 %0, %1, %2 offset:%3 sc1" : "=&v"(d) : "v"(voff), "s"(base), "i"(OFF) : "memory");
}
template <int OFF> DI void ldc2(u32x2& d, const char* base, u32 voff) {
  asm volatile("global_load_dwordx2 %0, %1, %2 offset:%3 sc1" : "=&v"(d) : "v"(voff), "s"(base), "i"(OFF) : "memory");
}
DI void ldg4(u32x4& d, const char* base, u32 voff) {
  asm volatile("global_load_dwordx4 %0, %1, %2" : "=&v"(d) : "v"(voff), "s"(base) : "memory");
}
DI void ldg2(u32x2& d, const char* base, u32 voff) {
  asm volatile("global_load_dwordx2 %0, %1, %2" : "=&v"(d) : "v"(voff), "s"(base) : "memory");
}
DI void ldgh(u32& d, const char* base, u32 voff) {
  asm volatile("global_load_ushort %0, %1, %2" : "=&v"(d) : "v"(voff), "s"(base) : "memory");
}
DI void stc4(const char* base, u32 voff, u32x4 v) {
  asm volatile("global_store_dwordx4 %0, %1, %2 sc1\n\ts_nop 1" ::"v"(voff), "v"(v), "s"(base) : "memory");
}
DI void stc2(const char* base, u32 voff, u32x2 v) {
  asm volatile("global_store_dwordx2 %0, %1, %2 sc1" ::"v"(voff), "v"(v), "s"(base) : "memory");
}
DI void stc1(const char* base, u32 voff, u32 v) {
  asm volatile("global_store_dword %0, %1, %2 sc1" ::"v"(voff), "v"(v), "s"(base) : "memory");
}

DI u32x4 lr4(u32 a) { return *(LAS u32x4*)a; }
DI void lw4(u32 a, u32x4 v) { *(LAS u32x4*)a = v; }
DI void lw2(u32 a, u32x2 v) { *(LAS u32x2*)a = v; }
DI void lw1(u32 a, u32 v) { *(LAS u32*)a = v; }
DI u32x4 ltr(u32 a0, u32 a1) {
  u32x2 p = __builtin_bit_cast(u32x2, __builtin_amdgcn_ds_read_tr16_b64_v4i16((LAS s16x4*)a0));
  u32x2 q = __builtin_bit_cast(u32x2, __builtin_amdgcn_ds_read_tr16_b64_v4i16((LAS s16x4*)a1));
  return u32x4{p.x, p.y, q.x, q.y};
}
DI f32x4 mfma(u32x4 a, u32x4 b, f32x4 c) {
  return __builtin_amdgcn_mfma_f32_16x16x32_bf16(__builtin_bit_cast(bf16x8, a), __builtin_bit_cast(bf16x8, b), c, 0, 0, 0);
}
DI u32 fu(float f) { return __float_as_uint(f); }
DI float uf(u32 u) { return __uint_as_float(u); }
DI u32 pk(float a, float b) { return __builtin_bit_cast(u32, __builtin_convertvector(f32x2{a, b}, bf16x2)); }
DI float lo(u32 u) { return uf(u << 16); }
DI float hi(u32 u) { return uf(u & 0xffff0000u); }
DI float rbf(float a) { return lo(pk(a, a)); }
DI u32 canon(u32 u) { return u < SENT - 1 ? u : SENT - 1; }
DI u32x4 canon4(f32x4 v) { return u32x4{canon(fu(v.x)), canon(fu(v.y)), canon(fu(v.z)), canon(fu(v.w))}; }
#pragma float_control(push)
#pragma float_control(precise, off)
DI float fmx(float a, float b) { return __builtin_fmaxf(a, b); }
#pragma float_control(pop)
DI u32 umax(u32 a, u32 b) { return a > b ? a : b; }
DI u32 umax4(u32x4 v) { return umax(umax(v.x, v.y), umax(v.z, v.w)); }
template <int C> DI float ror(float v) { return uf((u32)__builtin_amdgcn_update_dpp(0, (int)fu(v), 0x120 + C, 0xf, 0xf, true)); }
template <int BANKS> DI u32 ror8m(u32 keep, u32 src) { return (u32)__builtin_amdgcn_update_dpp((int)keep, (int)src, 0x128, 0xf, BANKS, false); }
DI float add32(float a, float b) { auto s = __builtin_amdgcn_permlane32_swap(fu(a), fu(b), false, false); return uf(s[0]) + uf(s[1]); }
DI float add16(float a, float b) { auto s = __builtin_amdgcn_permlane16_swap(fu(a), fu(b), false, false); return uf(s[0]) + uf(s[1]); }
DI float max32(float a) { auto s = __builtin_amdgcn_permlane32_swap(fu(a), fu(a), false, false); return fmx(uf(s[0]), uf(s[1])); }
DI float max16(float a) { auto s = __builtin_amdgcn_permlane16_swap(fu(a), fu(a), false, false); return fmx(uf(s[0]), uf(s[1])); }
DI float wsum(float v) {
  v += ror<8>(v); v += ror<4>(v); v += ror<2>(v); v += ror<1>(v);
  v = add16(v, v);
  return add32(v, v);
}
DI f32x4 sel(bool c, f32x4 a, f32x4 b) { return f32x4{c ? a.x : b.x, c ? a.y : b.y, c ? a.z : b.z, c ? a.w : b.w}; }
DI void lds_signal(u32 a) {
  asm volatile("s_waitcnt lgkmcnt(0)" ::: "memory");
  lw1(a, 1u);
}
DI void lds_wait4(u32 a, u32& spins) {
  for (;;) {
    asm volatile("" ::: "memory");
    const u32x4 f = lr4(a);
    if (__builtin_amdgcn_readfirstlane(f.x & f.y & f.z & f.w) || ++spins > SPIN_MAX) break;
  }
  asm volatile("" ::: "memory");
}
DI bool none(bool bad) { return __builtin_amdgcn_ballot_w64(bad) == 0; }
DI float ex2(float v) { return __builtin_amdgcn_exp2f(v); }
DI float rcp(float v) { return __builtin_amdgcn_rcpf(v); }
template <int N> DI void nap() { if (N > 0) __builtin_amdgcn_s_sleep(N); }

DI void red8(const float (&a)[8], float& o0, float& o1) {
  float r0 = add32(a[0], a[4]), r1 = add32(a[1], a[5]), r2 = add32(a[2], a[6]), r3 = add32(a[3], a[7]);
  float s0 = add16(r0, r2), s1 = add16(r1, r3);
  o0 = s0 + ror<8>(s0);
  o1 = s1 + ror<8>(s1);
}
DI float red4(f32x4 a) {
  float r0 = add32(a.x, a.z), r1 = add32(a.y, a.w);
  float s = add16(r0, r1);
  return s + ror<8>(s);
}

#define STAMP(i)
#define STAMPW(i)

__global__ __launch_bounds__(512, 2) void mla_ffn_decode(Args a_unused) {
  unsigned long long tt0, tt1;
  asm volatile("s_memrealtime %0\n\ts_waitcnt lgkmcnt(0)" : "=s"(tt0));
  const __attribute__((address_space(4))) Args* kp = (const __attribute__((address_space(4))) Args*)__builtin_amdgcn_kernarg_segment_ptr();
  asm volatile("" : "+s"(kp));
  Args a;
  a.x = kp->x; a.ckv = kp->ckv; a.wq = kp->wq; a.wuk = kp->wuk; a.wuv = kp->wuv; a.wo = kp->wo; a.w1 = kp->w1; a.b1 = kp->b1; a.w2 = kp->w2; a.b2 = kp->b2; a.out = kp->out; a.ws = kp->ws;
  asm volatile("" :: "s"(a.ws), "s"(a.x));
  asm volatile("s_memrealtime %0\n\ts_waitcnt lgkmcnt(0)" : "=s"(tt1));

  const u32 tid = threadIdx.x, bid = blockIdx.x, lane = tid & 63;
  const u32 w = __builtin_amdgcn_readfirstlane(tid >> 6);
  STAMP(0);
  const u32 xcd = bid & 7, r = bid >> 3, grp = r & 1, j = (r >> 1) & 7, h = grp * 16 + xcd * 2 + (r >> 4);
  const u32 ab = xcd >> 1, aks = (xcd & 1) * 16 + (r >> 1), hg = 1 - grp, sp = ab * 32 + aks;
  const u32 mq = j >> 1, dh = j & 1;
  const u32 l15 = lane & 15, g = lane >> 4, bq = lane & 3, u = (lane >> 2) & 3, l7 = lane & 7, l3 = lane >> 3;
  const u32 tq = u, tp = bq;
  const bool ffn = w == 3 && h < 28, fin = w == 7 && h >= 24;
  if (w == 0) lw2(L_FL + lane * 8, u32x2{0, 0});
  BARRIER();
  const char* ws = a.ws;
  const u32 tag = *(const __attribute__((address_space(4))) u32*)ws + 1;
  u32 spins = 0;

  const u32 f1 = (l3 >> 1) & 1;
  const u32 trn = (g * 8 + tq) * 128 + (tp & 1) * 8, trf = ((tq >> 1) | ((g & 1) << 1)) << 1;
  const u32x4 sent4 = {SENT, SENT, SENT, SENT};
  u32 o5, ohf, ost, o3, o3b, bfr, va, frag[4];
  u32x4 xf0, xf1;
#define COMMON_ADDR()                                                                                        \
  do {                                                                                                       \
    o5 = (j * 8 + bq * 2 + (u >> 1)) * 4096 + h * 128 + (u & 1) * 64 + g * 16;                               \
    ohf = W_HF + h * 8192 + j * 1024 + lane * 16;                                                            \
    ost = W_ST + (hg * 16 + l15) * 2048 + sp * 16;                                                           \
    o3 = W_H3 + (((hg * 16 + l7) * 8 + w) * 128 + sp) * 128 + (((lane >> 3) & 1) * 4 + g) * 16;              \
    o3b = o3 + 8 * 8 * 16384;                                                                                \
    bfr = g * 64 + bq * 16;                                                                                  \
    va = (g * 4 + tq) * 1024 + (((w * 8 + (tp >> 1)) ^ (((g & 1) * 4 + tq) << 1)) << 4) + (tp & 1) * 8;      \
    _Pragma("unroll") for (int i = 0; i < 4; ++i) frag[i] = (l15 * 1024 + ((g ^ (l7 << 1)) << 4)) ^ (i << 6); \
    PIN(o5); PIN(ohf); PIN(ost); PIN(o3); PIN(o3b); PIN(bfr); PIN(va);                                       \
    PIN(frag[0]); PIN(frag[1]); PIN(frag[2]); PIN(frag[3]);                                                  \
  } while (0)
  const char* kvb = a.ckv + (size_t)(ab * 4096 + aks * 128 + (w & 3) * 32) * 1024;

  if (w < 4) {
    u32x4 xv;
    ldg4(xv, a.x, w * 1024 + lane * 16);
    const u32 vq = ((mq * 128 + w * 32 + l3) * 32 + h) * 256 + dh * 128;
    const u32 vq0 = vq + ((l7 ^ (f1 << 1)) << 4), vq1 = vq + 8 * 8192 + ((l7 ^ ((f1 | 2) << 1)) << 4);
    dma<1>(a.wq, vq0, L_X + w * 4096);
    dma<1>(a.wq, vq1, L_X + w * 4096 + 1024);
    dma<1>(a.wq + 16 * 8192, vq0, L_X + w * 4096 + 2048);
    dma<1>(a.wq + 16 * 8192, vq1, L_X + w * 4096 + 3072);
#define UK_DMA()                                                                                              \
  _Pragma("unroll") for (int i = 0; i < 4; ++i)                                                               \
      dma<1>(a.wuk + i * 4 * 8192, ((j * 64 + w * 16 + g) * 32 + h) * 256 + ((l15 ^ (i * 4 + g)) << 4), L_Y + w * 4096 + i * 1024)
    if (!P_UKL) { UK_DMA(); }
    COMMON_ADDR();
    u32 oc = w == 0 ? ohf : w == 1 ? W_H4 + h * 8192 + l3 * 1024 + j * 128 + l7 * 16 : w == 2 ? W_H5 + o5 : W_HY + o5;
    u32 taq = L_X + trn + (((2 * w + (tp >> 1)) ^ trf) << 4);
    u32 o1s = W_H1 + h * 16384 + dh * 8192 + w * 2048 + mq * 512 + (g * 8 + l7) * 16;
    u32 o1 = W_H1 + h * 16384 + w * 4096 + lane * 16;
    u32 iq = L_Q0 + (4 * w + (lane >> 5) * 2 + (g & 1)) * 64 + bq * 16 + (l3 & 1) * 8 + (u & 1) * 4;
    u32 ua = L_Y + (w * 32 + l15) * 256 + ((g ^ l15) << 4);
    u32 o2s = W_H2 + (bq * 32 + h) * 2048 + j * 256 + w * 128 + u * 64 + g * 16;
    u32 o2 = W_H2 + (ab * 32 + hg * 16 + 4 * w) * 2048 + lane * 16, o2b = o2 + 4096;
    u32 i0 = L_X + 4 * w * 1024 + (((lane >> 1) ^ (((4 * w) & 7) << 1)) << 4) + (lane & 1) * 8;
    PIN(oc); PIN(taq); PIN(o1s); PIN(o1); PIN(iq); PIN(ua); PIN(o2s); PIN(o2); PIN(o2b); PIN(i0);

    WAITV(P_UKL ? 4 : 8);
    STAMP(19);
    PIN(xv);
    {
      float v[8] = {lo(xv.x), hi(xv.x), lo(xv.y), hi(xv.y), lo(xv.z), hi(xv.z), lo(xv.w), hi(xv.w)};
      const float mean = wsum(((v[0] + v[1]) + (v[2] + v[3])) + ((v[4] + v[5]) + (v[6] + v[7]))) * (1.f / 512.f);
      float d[8];
#pragma unroll
      for (int i = 0; i < 8; ++i) { d[i] = v[i] - mean; d[i] *= d[i]; }
      const float var = rbf(wsum(((d[0] + d[1]) + (d[2] + d[3])) + ((d[4] + d[5]) + (d[6] + d[7]))) * (1.f / 512.f)), mb = rbf(mean);
      const float inv = rcp(rbf(__builtin_amdgcn_sqrtf(rbf(var + 1e-5f))));
      u32x4 o;
#pragma unroll
      for (int i = 0; i < 4; ++i) { const u32 c = pk(v[2 * i] - mb, v[2 * i + 1] - mb); o[i] = pk(lo(c) * inv, hi(c) * inv); }
      PIN(o);
      if (g == mq) lw4(L_XN + l15 * 64 + w * 16, o);
    }
    WAITV(P_UKL ? 0 : 4);
    STAMP(1);
    lds_signal(L_FL + w * 4);
    if (P_UKL == 1) { UK_DMA(); }
    lds_wait4(L_FL, spins);
    {
      u32x4 fa[4], fb[4];
#pragma unroll
      for (int ks = 0; ks < 4; ++ks) { fa[ks] = ltr(taq + ks * 4096, taq + ks * 4096 + 512); fb[ks] = lr4(L_XN + ks * 256 + bfr); }
      FENCE();
      f32x4 c0 = mfma(fa[0], fb[0], f32x4{0, 0, 0, 0}), c1 = mfma(fa[1], fb[1], f32x4{0, 0, 0, 0});
      c0 = mfma(fa[2], fb[2], c0);
      c1 = mfma(fa[3], fb[3], c1);
      xf0 = dh ? fb[2] : fb[0];
      xf1 = dh ? fb[3] : fb[1];
      const f32x4 acc = c0 + c1;
      u32x4 o = {fu((u & 1) ? acc.z : acc.x), tag, fu((u & 1) ? acc.w : acc.y), tag};
      PIN(o);
      if (u < 2) stc4(ws, o1s, o);
    }
    STAMP(2);
    if (P_ST == 1) lw1(L_FL + 32 + w * 4, 1u);
    if (P_UKL == 2) { UK_DMA(); }
    stc4(ws, oc, sent4);
    {
      u32x4 p0, p1, p2, p3;
      nap<P_D1>();
      for (;;) {
        ldc4<0>(p0, ws, o1); ldc4<1024>(p1, ws, o1); ldc4<2048>(p2, ws, o1); ldc4<3072>(p3, ws, o1);
        if (P_ST == 2) lw1(L_FL + 32 + w * 4, 1u);
        WAITV(0);
        PIN(p0); PIN(p1); PIN(p2); PIN(p3);
        const u32 bad = (p0.y ^ tag) | (p0.w ^ tag) | (p1.y ^ tag) | (p1.w ^ tag) | (p2.y ^ tag) | (p2.w ^ tag) | (p3.y ^ tag) | (p3.w ^ tag);
        if (none(bad != 0) || ++spins > SPIN_MAX) break;
      }
      lw1(iq, pk(add32(uf(p0.x) + uf(p1.x), uf(p2.x) + uf(p3.x)), add32(uf(p0.z) + uf(p1.z), uf(p2.z) + uf(p3.z))));
    }
    STAMP(3);
    if (P_ST == 3) lw1(L_FL + 32 + w * 4, 1u);
    lds_signal(L_FL + 16 + w * 4);
    if (w < 2) {
      u32x4 fb[4], f0[4], f1v[4];
      lds_wait4(L_FL + 16, spins);
#pragma unroll
      for (int ks = 0; ks < 4; ++ks) { fb[ks] = lr4(L_Q0 + bfr + ks * 256); f0[ks] = lr4(ua ^ (ks << 6)); f1v[ks] = lr4((ua ^ (ks << 6)) + 4096); }
      FENCE();
      f32x4 c0 = {0, 0, 0, 0}, c1 = {0, 0, 0, 0};
#pragma unroll
      for (int ks = 0; ks < 4; ++ks) { c0 = mfma(f0[ks], fb[ks], c0); c1 = mfma(f1v[ks], fb[ks], c1); }
      const float sc = 0.08838834764831845f * 1.4426950408889634f;
      const f32x4 c = sel(u & 1, c1, c0) * sc;
      u32x4 o = {pk(c.x, c.y), tag, pk(c.z, c.w), tag};
      PIN(o);
      if (u < 2) stc4(ws, o2s, o);
      STAMP(4);
      nap<P_D2>();
    } else {
      lds_wait4(L_FL + 16, spins);
      nap<P_D2 + 6>();
    }
    u32x4 s[8];
    for (;;) {
      ldc4<0>(s[0], ws, o2); ldc4<1024>(s[1], ws, o2); ldc4<2048>(s[2], ws, o2); ldc4<3072>(s[3], ws, o2);
      ldc4<0>(s[4], ws, o2b); ldc4<1024>(s[5], ws, o2b); ldc4<2048>(s[6], ws, o2b); ldc4<3072>(s[7], ws, o2b);
      WAITV(0);
      u32 bad = 0;
#pragma unroll
      for (int i = 0; i < 8; ++i) { PIN(s[i]); bad |= (s[i].y ^ tag) | (s[i].w ^ tag); }
      if (none(bad != 0) || ++spins > SPIN_MAX) break;
    }
#pragma unroll
    for (int i = 0; i < 8; ++i) lw2((i0 ^ ((i >> 1) << 5)) + (i >> 1) * 1024 + (i & 1) * 512, u32x2{s[i].x, s[i].z});
    STAMP(5);
    BARRIER();
  } else {
    COMMON_ADDR();
    if (P_ST) lds_wait4(L_FL + 32, spins);
    nap<P_DS>();
    STAMP(2);
    const u32 lb = (w & 3) * 32768, off = grp ? P_OFF : 0;
#pragma unroll
    for (int i = 0; i < 31; ++i) {
      if (i >= P_W) WAITV(P_W - 1);
      const u32 row = i < 31 - P_OFF ? i + off : i - (31 - P_OFF) + (grp ? 0 : 31 - P_OFF);
      dma<KVNT>(kvb + row * 1024, (lane ^ ((row & 7) << 1)) << 4, lb + row * 1024);
    }
    STAMP(4);
    BARRIER();
    dma<KVNT>(kvb + 31 * 1024, (lane ^ 14) << 4, lb + 31 * 1024);
  }
  stc4(ws, o3, sent4);
  stc4(ws, o3b, sent4);
  const u32 lkv = w * 16384;
  const u32 vuk = ((j * 64 + w * 8 + g) * 32 + h) * 256;
  dma<1>(a.wuv, vuk + ((l15 ^ (g << 1)) << 4), L_Y + w * 2048);
  dma<1>(a.wuv, vuk + 4 * 8192 + ((l15 ^ ((4 + g) << 1)) << 4), L_Y + w * 2048 + 1024);

  u32x4 fr[16];
#pragma unroll
  for (int ks = 0; ks < 16; ++ks) fr[ks] = lr4(L_X + frag[ks & 3] + (ks >> 2) * 256);
#pragma unroll
  for (int i = 0; i < 4; ++i) { frag[i] += lkv; PIN(frag[i]); }
  STAMP(6);
  BARRIER();
  if (ffn) {
#pragma unroll
    for (int i = 0; i < 8; ++i)
      dma<1>(a.w1 + i * 8 * 3584, (j * 64 + l3) * 3584 + h * 128 + ((l7 ^ ((f1 | ((i & 1) << 1)) << 1)) << 4), L_W1 + i * 1024);
  }
  u32 imx = L_MX + l15 * 32, ip = L_P + lane * 16;
  PIN(imx); PIN(ip);
  if (w >= 4) WAITV(4);
  STAMP(7);
  BARRIER();

  f32x4 sv;
  {
    u32x4 fa[16];
#pragma unroll
    for (int ks = 0; ks < 16; ++ks) fa[ks] = lr4(frag[ks & 3] + (ks >> 2) * 256);
    FENCE();
    f32x4 c0 = {0, 0, 0, 0}, c1 = {0, 0, 0, 0};
#pragma unroll
    for (int ks = 0; ks < 16; ks += 2) { c0 = mfma(fa[ks], fr[ks], c0); c1 = mfma(fa[ks + 1], fr[ks + 1], c1); }
    sv = c0 + c1;
    float m = max32(max16(fmx(fmx(sv.x, sv.y), fmx(sv.z, sv.w))));
    PIN(m);
    if (g == 0) lw1(imx + w * 4, fu(m));
  }
  STAMP(8);
  BARRIER();
  float mx;
  {
    const u32x4 m0 = lr4(imx), m1 = lr4(imx + 16);
    FENCE();
#pragma unroll
    for (int ks = 0; ks < 4; ++ks)
#pragma unroll
      for (int t = 0; t < 4; ++t) fr[t * 4 + ks] = ltr((va ^ (t << 5)) + ks * 32768, (va ^ (t << 5)) + ks * 32768 + 16384);
    FENCE();
    mx = fmx(fmx(fmx(uf(m0.x), uf(m0.y)), fmx(uf(m0.z), uf(m0.w))), fmx(fmx(uf(m1.x), uf(m1.y)), fmx(uf(m1.z), uf(m1.w))));
    const u32 p0 = pk(ex2(sv.x - mx), ex2(sv.y - mx)), p1 = pk(ex2(sv.z - mx), ex2(sv.w - mx));
    lw2(ip + (w >> 1) * 1024 + (w & 1) * 8, u32x2{p0, p1});
    float l = (lo(p0) + hi(p0)) + (lo(p1) + hi(p1));
    l = add16(l, l);
    l = add32(l, l);
    PIN(l);
    if (g == 0) lw1(imx + (L_LS - L_MX) + w * 4, fu(l));
    if (ffn) WAITV(10); else WAITV(2);
  }
  STAMP(9);
  BARRIER();
#define WO_DMA()                                                                          \
  do {                                                                                    \
    if (ffn) {                                                                            \
      _Pragma("unroll") for (int i = 0; i < 8; ++i)                                       \
          dma<1>(a.w2 + i * 8192, (h * 64 + l3) * 1024 + j * 128 + ((l7 ^ (l3 & 6)) << 4), L_W2 + i * 1024); \
    }                                                                                     \
    const u32 vo = (h * 128 + w * 16 + l3) * 1024 + j * 128;                              \
    dma<1>(a.wo, vo + ((l7 ^ (f1 << 1)) << 4), L_WO + w * 2048);                          \
    dma<1>(a.wo, vo + 8 * 1024 + ((l7 ^ ((f1 | 2) << 1)) << 4), L_WO + w * 2048 + 1024);  \
  } while (0)
  {
    u32x4 pf[4];
#pragma unroll
    for (int ks = 0; ks < 4; ++ks) pf[ks] = lr4(ip + ks * 1024);
    if (w == 7) {
      const u32x4 l0 = lr4(imx + (L_LS - L_MX)), l1 = lr4(imx + (L_LS - L_MX) + 16);
      u32x4 o = {fu(mx), tag, fu(((uf(l0.x) + uf(l0.y)) + (uf(l0.z) + uf(l0.w))) + ((uf(l1.x) + uf(l1.y)) + (uf(l1.z) + uf(l1.w)))), tag};
      PIN(o);
      if (lane < 16) stc4(ws, ost, o);
    }
    FENCE();
    f32x4 o[4] = {{0, 0, 0, 0}, {0, 0, 0, 0}, {0, 0, 0, 0}, {0, 0, 0, 0}};
#pragma unroll
    for (int ks = 0; ks < 4; ++ks)
#pragma unroll
      for (int t = 0; t < 4; ++t) o[t] = mfma(fr[t * 4 + ks], pf[ks], o[t]);
    const u32x4 r0 = {pk(o[0].x, o[0].y), pk(o[0].z, o[0].w), pk(o[1].x, o[1].y), pk(o[1].z, o[1].w)};
    const u32x4 r1 = {pk(o[2].x, o[2].y), pk(o[2].z, o[2].w), pk(o[3].x, o[3].y), pk(o[3].z, o[3].w)};
    u32x4 n0, n1;
#pragma unroll
    for (int e = 0; e < 4; ++e) { n0[e] = canon(ror8m<0xC>(r0[e], r1[e])); n1[e] = canon(ror8m<0x3>(r1[e], r0[e])); }
    stc4(ws, o3, n0);
    stc4(ws, o3b, n1);
  }
  STAMP(10);
  if (P_WO == 0) WO_DMA();

  const u32 fb = (h >> 1) & 3, fm = j * 64 + (h & 1) * 32 + l7 * 4 + (lane >> 5) * 2 + ((lane >> 4) & 1);
  u32 osn = W_ST + h * 2048 + ((w >> 1) * 32 + l3) * 16 + (w & 1) * 256, oso = osn ^ 256;
  u32 od = W_H3 + ((h * 8 + j) * 128 + w * 16) * 128 + lane * 16;
  u32 icb = L_CB + l7 * 128 + ((((w & 1) * 4 + (w >> 1)) ^ l7) << 4) + (lane >> 5) * 8 + ((lane >> 4) & 1) * 4;
  u32 vu = L_Y + (g * 4 + tq) * 256 + (((2 * w + (tp >> 1)) ^ (((g & 1) * 4 + tq) << 1)) << 4) + (tp & 1) * 8;
  u32 rcb = L_CB + g * 128 + ((l7 ^ g) << 4);
  u32 o4s = W_H4 + h * 8192 + w * 1024 + j * 128 + bq * 32 + g * 8;
  PIN(osn); PIN(oso); PIN(od); PIN(icb); PIN(vu); PIN(rcb); PIN(o4s);
  if (ffn) {
    WAITV(P_WO == 0 ? 12 : 2);
    u32x4 fa[8];
#pragma unroll
    for (int t = 0; t < 4; ++t) {
      const u32 ta = L_W1 + trn + (((2 * t + (tp >> 1)) ^ trf) << 4);
      fa[2 * t] = ltr(ta, ta + 512);
      fa[2 * t + 1] = ltr(ta + 4096, ta + 4096 + 512);
    }
    FENCE();
    f32x4 d[4];
#pragma unroll
    for (int t = 0; t < 4; ++t) d[t] = mfma(fa[2 * t], xf0, f32x4{0, 0, 0, 0});
#pragma unroll
    for (int t = 0; t < 4; ++t) d[t] = mfma(fa[2 * t + 1], xf1, d[t]);
    stc4(ws, ohf, canon4(sel(u & 2, sel(u & 1, d[3], d[2]), sel(u & 1, d[1], d[0]))));
    nap<(P_D3 > 10 ? P_D3 - 10 : 0)>();
  } else {
    nap<P_D3>();
  }
  {
    u32x4 e0, e1, e2, e3, d0, d1;
    STAMP(11);
    for (;;) {
      ldc4<0>(e0, ws, osn); ldc4<128>(e1, ws, osn); ldc4<0>(e2, ws, oso); ldc4<128>(e3, ws, oso);
      WAITV(0);
      PIN(e0); PIN(e1); PIN(e2); PIN(e3);
      const u32 bad = (e0.y ^ tag) | (e0.w ^ tag) | (e1.y ^ tag) | (e1.w ^ tag) | (e2.y ^ tag) | (e2.w ^ tag) | (e3.y ^ tag) | (e3.w ^ tag);
      if (none(bad != 0) || ++spins > SPIN_MAX) break;
    }
    STAMP(17);
    nap<P_D3B>();
    ldc4<0>(d0, ws, od); ldc4<1024>(d1, ws, od);
    if (P_WO == 1) WO_DMA();
    float m = fmx(fmx(uf(e0.x), uf(e1.x)), fmx(uf(e2.x), uf(e3.x)));
    m = max32(max16(fmx(m, ror<8>(m))));
    float w0 = ex2(uf(e0.x) - m), w1 = ex2(uf(e1.x) - m);
    float l = w0 * uf(e0.z) + w1 * uf(e1.z) + ex2(uf(e2.x) - m) * uf(e2.z) + ex2(uf(e3.x) - m) * uf(e3.z);
    l += ror<8>(l);
    l = add16(l, l);
    l = add32(l, l);
    const float il = rcp(l);
    w0 *= il; w1 *= il;
    PIN(w0); PIN(w1);
    if (P_WO != 1) WAITV(0); else if (ffn) WAITV(10); else WAITV(2);
    for (;;) {
      PIN(d0); PIN(d1);
      if (none(umax(umax4(d0), umax4(d1)) == SENT) || ++spins > SPIN_MAX) break;
      ldc4<0>(d0, ws, od); ldc4<1024>(d1, ws, od);
      WAITV(0);
    }
    STAMP(12);
    if (P_WO == 2) WO_DMA();
    float acc[8];
#pragma unroll
    for (int e = 0; e < 4; ++e) {
      acc[2 * e] = w0 * lo(d0[e]) + w1 * lo(d1[e]);
      acc[2 * e + 1] = w0 * hi(d0[e]) + w1 * hi(d1[e]);
    }
    float c0, c1;
    red8(acc, c0, c1);
    u32 o = pk(c0, c1);
    PIN(o);
    if (!(lane & 8)) lw1(icb, o);
  }
  STAMP(13);
  BARRIER();
  {
    const u32x4 a0 = ltr(vu, vu + 4096), b0 = lr4(rcb), a1 = ltr(vu + 8192, vu + 8192 + 4096), b1 = lr4((rcb ^ 64) + 512);
    FENCE();
    const f32x4 c = mfma(a0, b0, f32x4{0, 0, 0, 0}) + mfma(a1, b1, f32x4{0, 0, 0, 0});
    u32x2 v = {canon(pk(c.x + ror<4>(c.x), c.y + ror<4>(c.y))), canon(pk(c.z + ror<4>(c.z), c.w + ror<4>(c.w)))};
    PIN(v);
    if (u == 0) stc2(ws, o4s, v);
  }
  STAMP(14);
  if (P_WO != 0) { WAITV(1); BARRIER(); }
  if (w < 2) {
    const u32 t0 = L_WO + trn + (((4 * w + (tp >> 1)) ^ trf) << 4), t1 = t0 ^ 32;
    u32 o4 = W_H4 + h * 8192 + (g >> 1) * 1024 + u * 128 + bq * 32 + (g & 1) * 16, o4b = o4 + 4096;
    u32 o5s = W_H5 + (j * 8 + bq * 2 + w) * 4096 + h * 128 + u * 64 + g * 16;
    u32x4 fa[8];
#pragma unroll
    for (int ks = 0; ks < 4; ++ks) { fa[ks] = ltr(t0 + ks * 4096, t0 + ks * 4096 + 512); fa[4 + ks] = ltr(t1 + ks * 4096, t1 + ks * 4096 + 512); }
#pragma unroll
    for (int i = 0; i < 8; ++i) PIN(fa[i]);
    PIN(o4); PIN(o4b); PIN(o5s);
    nap<P_D4>();
    u32x4 p[8];
    for (;;) {
      ldc4<0>(p[0], ws, o4); ldc4<512>(p[1], ws, o4); ldc4<2048>(p[2], ws, o4); ldc4<2560>(p[3], ws, o4);
      ldc4<0>(p[4], ws, o4b); ldc4<512>(p[5], ws, o4b); ldc4<2048>(p[6], ws, o4b); ldc4<2560>(p[7], ws, o4b);
      WAITV(0);
      u32 t = 0;
#pragma unroll
      for (int i = 0; i < 8; ++i) { PIN(p[i]); t = umax(t, umax4(p[i])); }
      if (none(t == SENT) || ++spins > SPIN_MAX) break;
    }
    STAMP(15);
    f32x4 c[4] = {{0, 0, 0, 0}, {0, 0, 0, 0}, {0, 0, 0, 0}, {0, 0, 0, 0}};
#pragma unroll
    for (int ks = 0; ks < 4; ++ks) {
      c[0] = mfma(fa[ks], p[2 * ks], c[0]);
      c[1] = mfma(fa[4 + ks], p[2 * ks], c[1]);
      c[2] = mfma(fa[ks], p[2 * ks + 1], c[2]);
      c[3] = mfma(fa[4 + ks], p[2 * ks + 1], c[3]);
    }
    f32x4 c0 = c[0] + c[2], c1 = c[1] + c[3];
#pragma unroll
    for (int e = 0; e < 4; ++e) {
      c0[e] += ror<8>(c0[e]); c1[e] += ror<8>(c1[e]);
      c0[e] += ror<4>(c0[e]); c1[e] += ror<4>(c1[e]);
    }
    u32x4 o = canon4(sel(u & 1, c1, c0));
    PIN(o);
    if (u < 2) stc4(ws, o5s, o);
  } else if (ffn) {
    const u32 of = W_HF + h * 8192 + lane * 16;
    const u32 f2 = (((g & 1) << 1) | (tq >> 1)) << 1;
    u32x4 p[8], fa[8];
    u32x2 b1v;
    ldg2(b1v, a.b1, (h * 64 + u * 16 + g * 4) * 2);
    for (;;) {
      ldc4<0>(p[0], ws, of); ldc4<1024>(p[1], ws, of); ldc4<2048>(p[2], ws, of); ldc4<3072>(p[3], ws, of);
      ldc4<0>(p[4], ws, of + 4096); ldc4<1024>(p[5], ws, of + 4096); ldc4<2048>(p[6], ws, of + 4096); ldc4<3072>(p[7], ws, of + 4096);
#pragma unroll
      for (int t = 0; t < 4; ++t) {
        const u32 ta = L_W2 + (g * 4 + tq) * 128 + (((2 * t + (tp >> 1)) ^ f2) << 4) + (tp & 1) * 8;
        fa[2 * t] = ltr(ta, ta + 2048);
        fa[2 * t + 1] = ltr(ta + 4096, ta + 4096 + 2048);
      }
      WAITV(0);
      u32 t = 0;
#pragma unroll
      for (int i = 0; i < 8; ++i) { PIN(p[i]); t = umax(t, umax4(p[i])); }
      if (none(t == SENT) || ++spins > SPIN_MAX) break;
    }
    PIN(b1v);
    const float bb[4] = {lo(b1v.x), hi(b1v.x), lo(b1v.y), hi(b1v.y)};
    float ge[4];
#pragma unroll
    for (int e = 0; e < 4; ++e) {
      const float s = ((uf(p[0][e]) + uf(p[1][e])) + (uf(p[2][e]) + uf(p[3][e]))) + ((uf(p[4][e]) + uf(p[5][e])) + (uf(p[6][e]) + uf(p[7][e])));
      const float t = rbf(rbf(s) + bb[e]);
      const float z = ex2(2.302208198f * (t + 0.044715f * t * t * t));
      ge[e] = t - t * rcp(z + 1.f);
    }
    lw2(L_HB + (u >> 1) * 256 + bfr + (u & 1) * 8, u32x2{pk(ge[0], ge[1]), pk(ge[2], ge[3])});
    asm volatile("s_waitcnt lgkmcnt(0)" ::: "memory");
    const u32x4 hb0 = lr4(L_HB + bfr), hb1 = lr4(L_HB + 256 + bfr);
    f32x4 d[4];
#pragma unroll
    for (int t = 0; t < 4; ++t) d[t] = mfma(fa[2 * t], hb0, f32x4{0, 0, 0, 0});
#pragma unroll
    for (int t = 0; t < 4; ++t) d[t] = mfma(fa[2 * t + 1], hb1, d[t]);
    stc4(ws, W_HY + o5, canon4(sel(u & 2, sel(u & 1, d[3], d[2]), sel(u & 1, d[1], d[0]))));
    STAMP(15);
  } else if (fin) {
    u32 fy = W_HY + (j * 8 + (h & 7)) * 4096 + lane * 16, fy3 = lane < 32 ? fy + 3072 : fy, fs = fy + (W_H5 - W_HY);
    u32 oo = (fb * 512 + fm) * 2;
    u32 xo, b2o;
    ldgh(xo, a.x, oo);
    ldgh(b2o, a.b2, fm * 2);
    PIN(fy); PIN(fy3); PIN(fs); PIN(oo);
    nap<P_D5>();
    u32x4 p0, p1, p2, p3;
    for (;;) {
      ldc4<0>(p0, ws, fy); ldc4<1024>(p1, ws, fy); ldc4<2048>(p2, ws, fy); ldc4<0>(p3, ws, fy3);
      WAITV(0);
      PIN(p0); PIN(p1); PIN(p2); PIN(p3);
      if (none(umax(umax(umax4(p0), umax4(p1)), umax(umax4(p2), umax4(p3))) == SENT) || ++spins > SPIN_MAX) break;
    }
    STAMP(15);
    u32x4 q0, q1, q2, q3;
    ldc4<0>(q0, ws, fs); ldc4<1024>(q1, ws, fs); ldc4<2048>(q2, ws, fs); ldc4<3072>(q3, ws, fs);
    PIN(xo); PIN(b2o);
    const f32x4 z = {0, 0, 0, 0};
    const f32x4 sy = __builtin_bit_cast(f32x4, p0) + __builtin_bit_cast(f32x4, p1) + __builtin_bit_cast(f32x4, p2) + sel(lane < 32, __builtin_bit_cast(f32x4, p3), z);
    const float y = rbf(rbf(red4(sy)) + lo(b2o));
    float gate = rbf(y * rcp(1.f + ex2(-1.4426950408889634f * y)));
    PIN(gate);
    WAITV(0);
    for (;;) {
      PIN(q0); PIN(q1); PIN(q2); PIN(q3);
      if (none(umax(umax(umax4(q0), umax4(q1)), umax(umax4(q2), umax4(q3))) == SENT) || ++spins > SPIN_MAX) break;
      ldc4<0>(q0, ws, fs); ldc4<1024>(q1, ws, fs); ldc4<2048>(q2, ws, fs); ldc4<3072>(q3, ws, fs);
      WAITV(0);
    }
    const f32x4 s = (__builtin_bit_cast(f32x4, q0) + __builtin_bit_cast(f32x4, q1)) + (__builtin_bit_cast(f32x4, q2) + __builtin_bit_cast(f32x4, q3));
    const float o = lo(xo) + rbf(rbf(red4(s)) * gate);
    if (!(lane & 8)) *(u16*)(a.out + oo) = (u16)pk(o, o);
    if (h == 24 && j == 0 && lane == 0) stc1(ws, 0, tag);
  }
  if (fin) { const u32 t = (u32)__builtin_amdgcn_s_memrealtime(), d = (AMP == 1 ? (u32)tt1 - (u32)tt0 : t - (u32)tt0) * 8; while ((u32)__builtin_amdgcn_s_memrealtime() - t < (d < 40000 ? d : 40000)) {} }
}

extern "C" void kernel_launch(void* const* d_in, const int* in_sizes, int n_in, void* d_out, int out_size, void* d_ws, size_t ws_size, hipStream_t stream) {
  Args a;
  a.x = (const char*)d_in[0];
  a.ckv = (const char*)d_in[1];
  a.wq = (const char*)d_in[2];
  a.wuk = (const char*)d_in[3];
  a.wuv = (const char*)d_in[4];
  a.wo = (const char*)d_in[5];
  a.w1 = (const char*)d_in[7];
  a.b1 = (const char*)d_in[8];
  a.w2 = (const char*)d_in[9];
  a.b2 = (const char*)d_in[10];
  a.out = (char*)d_out;
  a.ws = (char*)d_ws;
  (void)hipFuncSetAttribute((const void*)mla_ffn_decode, hipFuncAttributeMaxDynamicSharedMemorySize, L_TOTAL);
  mla_ffn_decode<<<256, 512, L_TOTAL, stream>>>(a);
}
```

```cpp
#include <hip/hip_runtime.h>
#include <hip/hip_bf16.h>
#include <stdint.h>

typedef unsigned u32;
typedef unsigned short u16;
typedef u32 u32x2 __attribute__((ext_vector_type(2)));
typedef u32 u32x4 __attribute__((ext_vector_type(4)));
typedef float f32x2 __attribute__((ext_vector_type(2)));
typedef float f32x4 __attribute__((ext_vector_type(4)));
typedef __bf16 bf16x2 __attribute__((ext_vector_type(2)));
typedef __bf16 bf16x8 __attribute__((ext_vector_type(8)));
typedef short s16x4 __attribute__((ext_vector_type(4)));
#define LAS __attribute__((address_space(3)))
#define DI __device__ __forceinline__

#define OUTPOL "sc1"
#define P_W 6
#define P_ST 1
#define P_OFF 0
#define P_DS 0
#define KVNT 0
#define P_D1 12
#define P_D2 14
#define P_D3 8
#define P_D4 12
#define P_D5 40
#define P_WO 1
#define P_UKL 1
#define P_D3B 0
constexpr u32 SPIN_MAX = 1u << 16;
constexpr u32 SENT = 0xFFFFFFFFu;

constexpr u32 W_H1 = 0x1000, W_H2 = W_H1 + 0x80000, W_ST = W_H2 + 0x40000, W_HF = W_ST + 0x10000;
constexpr u32 W_H4 = W_HF + 0x40000, W_H5 = W_H4 + 0x40000, W_HY = W_H5 + 0x40000, W_H3 = W_HY + 0x40000;
constexpr u32 W_DG = W_H3 + 0x400000;

constexpr u32 L_X = 131072, L_Y = L_X + 16384, L_TOTAL = L_Y + 16384;
constexpr u32 L_Q0 = 31 * 1024, L_XN = 63 * 1024, L_FL = 95 * 1024;
constexpr u32 L_W1 = L_X, L_MX = L_X + 8192, L_LS = L_X + 8704, L_P = L_X + 9216, L_CB = L_X + 13312, L_CX = L_X + 14336, L_HB = L_X + 15360;
constexpr u32 L_WO = 0, L_W2 = 16384;

struct Args {
  const char *x, *ckv, *wq, *wuk, *wuv, *wo, *w1, *b1, *w2, *b2;
  char *out, *ws;
};

#define WAITV(n) asm volatile("s_waitcnt vmcnt(%0)" ::"i"(n) : "memory")
#define BARRIER() asm volatile("s_waitcnt lgkmcnt(0)\n\ts_barrier" ::: "memory")
#define PIN(v) asm volatile("" : "+v"(v))
#define FENCE() __builtin_amdgcn_sched_barrier(0)

template <int NT> DI void dma(const char* base, u32 voff, u32 lds) {
  u32 keep;
  lds = __builtin_amdgcn_readfirstlane(lds);
  if (NT)
    asm volatile("s_mov_b32 %0, m0\n\ts_mov_b32 m0, %3\n\ts_nop 0\n\tglobal_load_lds_dwordx4 %1, %2 nt\n\ts_mov_b32 m0, %0"
                 : "=&s"(keep) : "v"(voff), "s"(base), "s"(lds) : "memory");
  else
    asm volatile("s_mov_b32 %0, m0\n\ts_mov_b32 m0, %3\n\ts_nop 0\n\tglobal_load_lds_dwordx4 %1, %2\n\ts_mov_b32 m0, %0"
                 : "=&s"(keep) : "v"(voff), "s"(base), "s"(lds) : "memory");
}
template <int OFF> DI void ldc4(u32x4& d, const char* base, u32 voff) {
  asm volatile("global_load_dwordx4 %0, %1, %2 offset:%3 sc1" : "=&v"(d) : "v"(voff), "s"(base), "i"(OFF) : "memory");
}
template <int OFF> DI void ldc2(u32x2& d, const char* base, u32 voff) {
  asm volatile("global_load_dwordx2 %0, %1, %2 offset:%3 sc1" : "=&v"(d) : "v"(voff), "s"(base), "i"(OFF) : "memory");
}
DI void ldg4(u32x4& d, const char* base, u32 voff) {
  asm volatile("global_load_dwordx4 %0, %1, %2" : "=&v"(d) : "v"(voff), "s"(base) : "memory");
}
DI void ldg2(u32x2& d, const char* base, u32 voff) {
  asm volatile("global_load_dwordx2 %0, %1, %2" : "=&v"(d) : "v"(voff), "s"(base) : "memory");
}
DI void ldgh(u32& d, const char* base, u32 voff) {
  asm volatile("global_load_ushort %0, %1, %2" : "=&v"(d) : "v"(voff), "s"(base) : "memory");
}
DI void stc4(const char* base, u32 voff, u32x4 v) {
  asm volatile("global_store_dwordx4 %0, %1, %2 sc1\n\ts_nop 1" ::"v"(voff), "v"(v), "s"(base) : "memory");
}
DI void stc2(const char* base, u32 voff, u32x2 v) {
  asm volatile("global_store_dwordx2 %0, %1, %2 sc1" ::"v"(voff), "v"(v), "s"(base) : "memory");
}
DI void stc1(const char* base, u32 voff, u32 v) {
  asm volatile("global_store_dword %0, %1, %2 sc1" ::"v"(voff), "v"(v), "s"(base) : "memory");
}

DI u32x4 lr4(u32 a) { return *(LAS u32x4*)a; }
DI void lw4(u32 a, u32x4 v) { *(LAS u32x4*)a = v; }
DI void lw2(u32 a, u32x2 v) { *(LAS u32x2*)a = v; }
DI void lw1(u32 a, u32 v) { *(LAS u32*)a = v; }
DI u32x4 ltr(u32 a0, u32 a1) {
  u32x2 p = __builtin_bit_cast(u32x2, __builtin_amdgcn_ds_read_tr16_b64_v4i16((LAS s16x4*)a0));
  u32x2 q = __builtin_bit_cast(u32x2, __builtin_amdgcn_ds_read_tr16_b64_v4i16((LAS s16x4*)a1));
  return u32x4{p.x, p.y, q.x, q.y};
}
DI f32x4 mfma(u32x4 a, u32x4 b, f32x4 c) {
  return __builtin_amdgcn_mfma_f32_16x16x32_bf16(__builtin_bit_cast(bf16x8, a), __builtin_bit_cast(bf16x8, b), c, 0, 0, 0);
}
DI u32 fu(float f) { return __float_as_uint(f); }
DI float uf(u32 u) { return __uint_as_float(u); }
DI u32 pk(float a, float b) { return __builtin_bit_cast(u32, __builtin_convertvector(f32x2{a, b}, bf16x2)); }
DI float lo(u32 u) { return uf(u << 16); }
DI float hi(u32 u) { return uf(u & 0xffff0000u); }
DI float rbf(float a) { return lo(pk(a, a)); }
DI u32 canon(u32 u) { return u < SENT - 1 ? u : SENT - 1; }
DI u32x4 canon4(f32x4 v) { return u32x4{canon(fu(v.x)), canon(fu(v.y)), canon(fu(v.z)), canon(fu(v.w))}; }
#pragma float_control(push)
#pragma float_control(precise, off)
DI float fmx(float a, float b) { return __builtin_fmaxf(a, b); }
#pragma float_control(pop)
DI u32 umax(u32 a, u32 b) { return a > b ? a : b; }
DI u32 umax4(u32x4 v) { return umax(umax(v.x, v.y), umax(v.z, v.w)); }
template <int C> DI float ror(float v) { return uf((u32)__builtin_amdgcn_update_dpp(0, (int)fu(v), 0x120 + C, 0xf, 0xf, true)); }
template <int BANKS> DI u32 ror8m(u32 keep, u32 src) { return (u32)__builtin_amdgcn_update_dpp((int)keep, (int)src, 0x128, 0xf, BANKS, false); }
DI float add32(float a, float b) { auto s = __builtin_amdgcn_permlane32_swap(fu(a), fu(b), false, false); return uf(s[0]) + uf(s[1]); }
DI float add16(float a, float b) { auto s = __builtin_amdgcn_permlane16_swap(fu(a), fu(b), false, false); return uf(s[0]) + uf(s[1]); }
DI float max32(float a) { auto s = __builtin_amdgcn_permlane32_swap(fu(a), fu(a), false, false); return fmx(uf(s[0]), uf(s[1])); }
DI float max16(float a) { auto s = __builtin_amdgcn_permlane16_swap(fu(a), fu(a), false, false); return fmx(uf(s[0]), uf(s[1])); }
DI float wsum(float v) {
  v += ror<8>(v); v += ror<4>(v); v += ror<2>(v); v += ror<1>(v);
  v = add16(v, v);
  return add32(v, v);
}
DI f32x4 sel(bool c, f32x4 a, f32x4 b) { return f32x4{c ? a.x : b.x, c ? a.y : b.y, c ? a.z : b.z, c ? a.w : b.w}; }
DI void lds_signal(u32 a) {
  asm volatile("s_waitcnt lgkmcnt(0)" ::: "memory");
  lw1(a, 1u);
}
DI void lds_wait4(u32 a, u32& spins) {
  for (;;) {
    asm volatile("" ::: "memory");
    const u32x4 f = lr4(a);
    if (__builtin_amdgcn_readfirstlane(f.x & f.y & f.z & f.w) || ++spins > SPIN_MAX) break;
  }
  asm volatile("" ::: "memory");
}
DI bool none(bool bad) { return __builtin_amdgcn_ballot_w64(bad) == 0; }
DI float ex2(float v) { return __builtin_amdgcn_exp2f(v); }
DI float rcp(float v) { return __builtin_amdgcn_rcpf(v); }
template <int N> DI void nap() { if (N > 0) __builtin_amdgcn_s_sleep(N); }

DI void red8(const float (&a)[8], float& o0, float& o1) {
  float r0 = add32(a[0], a[4]), r1 = add32(a[1], a[5]), r2 = add32(a[2], a[6]), r3 = add32(a[3], a[7]);
  float s0 = add16(r0, r2), s1 = add16(r1, r3);
  o0 = s0 + ror<8>(s0);
  o1 = s1 + ror<8>(s1);
}
DI float red4(f32x4 a) {
  float r0 = add32(a.x, a.z), r1 = add32(a.y, a.w);
  float s = add16(r0, r1);
  return s + ror<8>(s);
}

#define STAMP(i)
#define STAMPW(i)

__global__ __launch_bounds__(512, 2) void mla_ffn_decode(Args a) {
  const u32 tid = threadIdx.x, bid = blockIdx.x, lane = tid & 63;
  const u32 w = __builtin_amdgcn_readfirstlane(tid >> 6);
  STAMP(0);
  const u32 xcd = bid & 7, r = bid >> 3, grp = r & 1, j = (r >> 1) & 7, h = grp * 16 + xcd * 2 + (r >> 4);
  const u32 ab = xcd >> 1, aks = (xcd & 1) * 16 + (r >> 1), hg = 1 - grp, sp = ab * 32 + aks;
  const u32 mq = j >> 1, dh = j & 1;
  const u32 l15 = lane & 15, g = lane >> 4, bq = lane & 3, u = (lane >> 2) & 3, l7 = lane & 7, l3 = lane >> 3;
  const u32 tq = u, tp = bq;
  const bool ffn = w == 3 && h < 28, fin = w == 7 && h >= 24;
  if (w == 0) lw2(L_FL + lane * 8, u32x2{0, 0});
  BARRIER();
  const char* ws = a.ws;
  const u32 tag = *(const __attribute__((address_space(4))) u32*)ws + 1;
  u32 spins = 0;

  const u32 f1 = (l3 >> 1) & 1;
  const u32 trn = (g * 8 + tq) * 128 + (tp & 1) * 8, trf = ((tq >> 1) | ((g & 1) << 1)) << 1;
  const u32x4 sent4 = {SENT, SENT, SENT, SENT};
  u32 o5, ohf, ost, o3, o3b, bfr, va, frag[4];
  u32x4 xf0, xf1;
#define COMMON_ADDR()                                                                                        \
  do {                                                                                                       \
    o5 = (j * 8 + bq * 2 + (u >> 1)) * 4096 + h * 128 + (u & 1) * 64 + g * 16;                               \
    ohf = W_HF + h * 8192 + j * 1024 + lane * 16;                                                            \
    ost = W_ST + (hg * 16 + l15) * 2048 + sp * 16;                                                           \
    o3 = W_H3 + (((hg * 16 + l7) * 8 + w) * 128 + sp) * 128 + (((lane >> 3) & 1) * 4 + g) * 16;              \
    o3b = o3 + 8 * 8 * 16384;                                                                                \
    bfr = g * 64 + bq * 16;                                                                                  \
    va = (g * 4 + tq) * 1024 + (((w * 8 + (tp >> 1)) ^ (((g & 1) * 4 + tq) << 1)) << 4) + (tp & 1) * 8;      \
    _Pragma("unroll") for (int i = 0; i < 4; ++i) frag[i] = (l15 * 1024 + ((g ^ (l7 << 1)) << 4)) ^ (i << 6); \
    PIN(o5); PIN(ohf); PIN(ost); PIN(o3); PIN(o3b); PIN(bfr); PIN(va);                                       \
    PIN(frag[0]); PIN(frag[1]); PIN(frag[2]); PIN(frag[3]);                                                  \
  } while (0)
  const char* kvb = a.ckv + (size_t)(ab * 4096 + aks * 128 + (w & 3) * 32) * 1024;

  if (w < 4) {
    u32x4 xv;
    ldg4(xv, a.x, w * 1024 + lane * 16);
    const u32 vq = ((mq * 128 + w * 32 + l3) * 32 + h) * 256 + dh * 128;
    const u32 vq0 = vq + ((l7 ^ (f1 << 1)) << 4), vq1 = vq + 8 * 8192 + ((l7 ^ ((f1 | 2) << 1)) << 4);
    dma<1>(a.wq, vq0, L_X + w * 4096);
    dma<1>(a.wq, vq1, L_X + w * 4096 + 1024);
    dma<1>(a.wq + 16 * 8192, vq0, L_X + w * 4096 + 2048);
    dma<1>(a.wq + 16 * 8192, vq1, L_X + w * 4096 + 3072);
#define UK_DMA()                                                                                              \
  _Pragma("unroll") for (int i = 0; i < 4; ++i)                                                               \
      dma<1>(a.wuk + i * 4 * 8192, ((j * 64 + w * 16 + g) * 32 + h) * 256 + ((l15 ^ (i * 4 + g)) << 4), L_Y + w * 4096 + i * 1024)
    if (!P_UKL) { UK_DMA(); }
    COMMON_ADDR();
    u32 oc = w == 0 ? ohf : w == 1 ? W_H4 + h * 8192 + l3 * 1024 + j * 128 + l7 * 16 : w == 2 ? W_H5 + o5 : W_HY + o5;
    u32 taq = L_X + trn + (((2 * w + (tp >> 1)) ^ trf) << 4);
    u32 o1s = W_H1 + h * 16384 + dh * 8192 + w * 2048 + mq * 512 + (g * 8 + l7) * 16;
    u32 o1 = W_H1 + h * 16384 + w * 4096 + lane * 16;
    u32 iq = L_Q0 + (4 * w + (lane >> 5) * 2 + (g & 1)) * 64 + bq * 16 + (l3 & 1) * 8 + (u & 1) * 4;
    u32 ua = L_Y + (w * 32 + l15) * 256 + ((g ^ l15) << 4);
    u32 o2s = W_H2 + (bq * 32 + h) * 2048 + j * 256 + w * 128 + u * 64 + g * 16;
    u32 o2 = W_H2 + (ab * 32 + hg * 16 + 4 * w) * 2048 + lane * 16, o2b = o2 + 4096;
    u32 i0 = L_X + 4 * w * 1024 + (((lane >> 1) ^ (((4 * w) & 7) << 1)) << 4) + (lane & 1) * 8;
    PIN(oc); PIN(taq); PIN(o1s); PIN(o1); PIN(iq); PIN(ua); PIN(o2s); PIN(o2); PIN(o2b); PIN(i0);

    WAITV(P_UKL ? 4 : 8);
    STAMP(19);
    PIN(xv);
    {
      float v[8] = {lo(xv.x), hi(xv.x), lo(xv.y), hi(xv.y), lo(xv.z), hi(xv.z), lo(xv.w), hi(xv.w)};
      const float mean = wsum(((v[0] + v[1]) + (v[2] + v[3])) + ((v[4] + v[5]) + (v[6] + v[7]))) * (1.f / 512.f);
      float d[8];
#pragma unroll
      for (int i = 0; i < 8; ++i) { d[i] = v[i] - mean; d[i] *= d[i]; }
      const float var = rbf(wsum(((d[0] + d[1]) + (d[2] + d[3])) + ((d[4] + d[5]) + (d[6] + d[7]))) * (1.f / 512.f)), mb = rbf(mean);
      const float inv = rcp(rbf(__builtin_amdgcn_sqrtf(rbf(var + 1e-5f))));
      u32x4 o;
#pragma unroll
      for (int i = 0; i < 4; ++i) { const u32 c = pk(v[2 * i] - mb, v[2 * i + 1] - mb); o[i] = pk(lo(c) * inv, hi(c) * inv); }
      PIN(o);
      if (g == mq) lw4(L_XN + l15 * 64 + w * 16, o);
    }
    WAITV(P_UKL ? 0 : 4);
    STAMP(1);
    lds_signal(L_FL + w * 4);
    if (P_UKL == 1) { UK_DMA(); }
    lds_wait4(L_FL, spins);
    {
      u32x4 fa[4], fb[4];
#pragma unroll
      for (int ks = 0; ks < 4; ++ks) { fa[ks] = ltr(taq + ks * 4096, taq + ks * 4096 + 512); fb[ks] = lr4(L_XN + ks * 256 + bfr); }
      FENCE();
      f32x4 c0 = mfma(fa[0], fb[0], f32x4{0, 0, 0, 0}), c1 = mfma(fa[1], fb[1], f32x4{0, 0, 0, 0});
      c0 = mfma(fa[2], fb[2], c0);
      c1 = mfma(fa[3], fb[3], c1);
      xf0 = dh ? fb[2] : fb[0];
      xf1 = dh ? fb[3] : fb[1];
      const f32x4 acc = c0 + c1;
      u32x4 o = {fu((u & 1) ? acc.z : acc.x), tag, fu((u & 1) ? acc.w : acc.y), tag};
      PIN(o);
      if (u < 2) stc4(ws, o1s, o);
    }
    STAMP(2);
    if (P_ST == 1) lw1(L_FL + 32 + w * 4, 1u);
    if (P_UKL == 2) { UK_DMA(); }
    stc4(ws, oc, sent4);
    {
      u32x4 p0, p1, p2, p3;
      nap<P_D1>();
      for (;;) {
        ldc4<0>(p0, ws, o1); ldc4<1024>(p1, ws, o1); ldc4<2048>(p2, ws, o1); ldc4<3072>(p3, ws, o1);
        if (P_ST == 2) lw1(L_FL + 32 + w * 4, 1u);
        WAITV(0);
        PIN(p0); PIN(p1); PIN(p2); PIN(p3);
        const u32 bad = (p0.y ^ tag) | (p0.w ^ tag) | (p1.y ^ tag) | (p1.w ^ tag) | (p2.y ^ tag) | (p2.w ^ tag) | (p3.y ^ tag) | (p3.w ^ tag);
        if (none(bad != 0) || ++spins > SPIN_MAX) break;
      }
      lw1(iq, pk(add32(uf(p0.x) + uf(p1.x), uf(p2.x) + uf(p3.x)), add32(uf(p0.z) + uf(p1.z), uf(p2.z) + uf(p3.z))));
    }
    STAMP(3);
    if (P_ST == 3) lw1(L_FL + 32 + w * 4, 1u);
    lds_signal(L_FL + 16 + w * 4);
    if (w < 2) {
      u32x4 fb[4], f0[4], f1v[4];
      lds_wait4(L_FL + 16, spins);
#pragma unroll
      for (int ks = 0; ks < 4; ++ks) { fb[ks] = lr4(L_Q0 + bfr + ks * 256); f0[ks] = lr4(ua ^ (ks << 6)); f1v[ks] = lr4((ua ^ (ks << 6)) + 4096); }
      FENCE();
      f32x4 c0 = {0, 0, 0, 0}, c1 = {0, 0, 0, 0};
#pragma unroll
      for (int ks = 0; ks < 4; ++ks) { c0 = mfma(f0[ks], fb[ks], c0); c1 = mfma(f1v[ks], fb[ks], c1); }
      const float sc = 0.08838834764831845f * 1.4426950408889634f;
      const f32x4 c = sel(u & 1, c1, c0) * sc;
      u32x4 o = {pk(c.x, c.y), tag, pk(c.z, c.w), tag};
      PIN(o);
      if (u < 2) stc4(ws, o2s, o);
      STAMP(4);
      nap<P_D2>();
    } else {
      lds_wait4(L_FL + 16, spins);
      nap<P_D2 + 6>();
    }
    u32x4 s[8];
    for (;;) {
      ldc4<0>(s[0], ws, o2); ldc4<1024>(s[1], ws, o2); ldc4<2048>(s[2], ws, o2); ldc4<3072>(s[3], ws, o2);
      ldc4<0>(s[4], ws, o2b); ldc4<1024>(s[5], ws, o2b); ldc4<2048>(s[6], ws, o2b); ldc4<3072>(s[7], ws, o2b);
      WAITV(0);
      u32 bad = 0;
#pragma unroll
      for (int i = 0; i < 8; ++i) { PIN(s[i]); bad |= (s[i].y ^ tag) | (s[i].w ^ tag); }
      if (none(bad != 0) || ++spins > SPIN_MAX) break;
    }
#pragma unroll
    for (int i = 0; i < 8; ++i) lw2((i0 ^ ((i >> 1) << 5)) + (i >> 1) * 1024 + (i & 1) * 512, u32x2{s[i].x, s[i].z});
    STAMP(5);
    BARRIER();
  } else {
    COMMON_ADDR();
    if (P_ST) lds_wait4(L_FL + 32, spins);
    nap<P_DS>();
    STAMP(2);
    const u32 lb = (w & 3) * 32768, off = grp ? P_OFF : 0;
#pragma unroll
    for (int i = 0; i < 31; ++i) {
      if (i >= P_W) WAITV(P_W - 1);
      const u32 row = i < 31 - P_OFF ? i + off : i - (31 - P_OFF) + (grp ? 0 : 31 - P_OFF);
      dma<KVNT>(kvb + row * 1024, (lane ^ ((row & 7) << 1)) << 4, lb + row * 1024);
    }
    STAMP(4);
    BARRIER();
    dma<KVNT>(kvb + 31 * 1024, (lane ^ 14) << 4, lb + 31 * 1024);
  }
  stc4(ws, o3, sent4);
  stc4(ws, o3b, sent4);
  const u32 lkv = w * 16384;
  const u32 vuk = ((j * 64 + w * 8 + g) * 32 + h) * 256;
  dma<1>(a.wuv, vuk + ((l15 ^ (g << 1)) << 4), L_Y + w * 2048);
  dma<1>(a.wuv, vuk + 4 * 8192 + ((l15 ^ ((4 + g) << 1)) << 4), L_Y + w * 2048 + 1024);

  u32x4 fr[16];
#pragma unroll
  for (int ks = 0; ks < 16; ++ks) fr[ks] = lr4(L_X + frag[ks & 3] + (ks >> 2) * 256);
#pragma unroll
  for (int i = 0; i < 4; ++i) { frag[i] += lkv; PIN(frag[i]); }
  STAMP(6);
  BARRIER();
  if (ffn) {
#pragma unroll
    for (int i = 0; i < 8; ++i)
      dma<1>(a.w1 + i * 8 * 3584, (j * 64 + l3) * 3584 + h * 128 + ((l7 ^ ((f1 | ((i & 1) << 1)) << 1)) << 4), L_W1 + i * 1024);
  }
  u32 imx = L_MX + l15 * 32, ip = L_P + lane * 16;
  PIN(imx); PIN(ip);
  if (w >= 4) WAITV(4);
  STAMP(7);
  BARRIER();

  f32x4 sv;
  {
    u32x4 fa[16];
#pragma unroll
    for (int ks = 0; ks < 16; ++ks) fa[ks] = lr4(frag[ks & 3] + (ks >> 2) * 256);
    FENCE();
    f32x4 c0 = {0, 0, 0, 0}, c1 = {0, 0, 0, 0};
#pragma unroll
    for (int ks = 0; ks < 16; ks += 2) { c0 = mfma(fa[ks], fr[ks], c0); c1 = mfma(fa[ks + 1], fr[ks + 1], c1); }
    sv = c0 + c1;
    float m = max32(max16(fmx(fmx(sv.x, sv.y), fmx(sv.z, sv.w))));
    PIN(m);
    if (g == 0) lw1(imx + w * 4, fu(m));
  }
  STAMP(8);
  BARRIER();
  float mx;
  {
    const u32x4 m0 = lr4(imx), m1 = lr4(imx + 16);
    FENCE();
#pragma unroll
    for (int ks = 0; ks < 4; ++ks)
#pragma unroll
      for (int t = 0; t < 4; ++t) fr[t * 4 + ks] = ltr((va ^ (t << 5)) + ks * 32768, (va ^ (t << 5)) + ks * 32768 + 16384);
    FENCE();
    mx = fmx(fmx(fmx(uf(m0.x), uf(m0.y)), fmx(uf(m0.z), uf(m0.w))), fmx(fmx(uf(m1.x), uf(m1.y)), fmx(uf(m1.z), uf(m1.w))));
    const u32 p0 = pk(ex2(sv.x - mx), ex2(sv.y - mx)), p1 = pk(ex2(sv.z - mx), ex2(sv.w - mx));
    lw2(ip + (w >> 1) * 1024 + (w & 1) * 8, u32x2{p0, p1});
    float l = (lo(p0) + hi(p0)) + (lo(p1) + hi(p1));
    l = add16(l, l);
    l = add32(l, l);
    PIN(l);
    if (g == 0) lw1(imx + (L_LS - L_MX) + w * 4, fu(l));
    if (ffn) WAITV(10); else WAITV(2);
  }
  STAMP(9);
  BARRIER();
#define WO_DMA()                                                                          \
  do {                                                                                    \
    if (ffn) {                                                                            \
      _Pragma("unroll") for (int i = 0; i < 8; ++i)                                       \
          dma<1>(a.w2 + i * 8192, (h * 64 + l3) * 1024 + j * 128 + ((l7 ^ (l3 & 6)) << 4), L_W2 + i * 1024); \
    }                                                                                     \
    const u32 vo = (h * 128 + w * 16 + l3) * 1024 + j * 128;                              \
    dma<1>(a.wo, vo + ((l7 ^ (f1 << 1)) << 4), L_WO + w * 2048);                          \
    dma<1>(a.wo, vo + 8 * 1024 + ((l7 ^ ((f1 | 2) << 1)) << 4), L_WO + w * 2048 + 1024);  \
  } while (0)
  {
    u32x4 pf[4];
#pragma unroll
    for (int ks = 0; ks < 4; ++ks) pf[ks] = lr4(ip + ks * 1024);
    if (w == 7) {
      const u32x4 l0 = lr4(imx + (L_LS - L_MX)), l1 = lr4(imx + (L_LS - L_MX) + 16);
      u32x4 o = {fu(mx), tag, fu(((uf(l0.x) + uf(l0.y)) + (uf(l0.z) + uf(l0.w))) + ((uf(l1.x) + uf(l1.y)) + (uf(l1.z) + uf(l1.w)))), tag};
      PIN(o);
      if (lane < 16) stc4(ws, ost, o);
    }
    FENCE();
    f32x4 o[4] = {{0, 0, 0, 0}, {0, 0, 0, 0}, {0, 0, 0, 0}, {0, 0, 0, 0}};
#pragma unroll
    for (int ks = 0; ks < 4; ++ks)
#pragma unroll
      for (int t = 0; t < 4; ++t) o[t] = mfma(fr[t * 4 + ks], pf[ks], o[t]);
    const u32x4 r0 = {pk(o[0].x, o[0].y), pk(o[0].z, o[0].w), pk(o[1].x, o[1].y), pk(o[1].z, o[1].w)};
    const u32x4 r1 = {pk(o[2].x, o[2].y), pk(o[2].z, o[2].w), pk(o[3].x, o[3].y), pk(o[3].z, o[3].w)};
    u32x4 n0, n1;
#pragma unroll
    for (int e = 0; e < 4; ++e) { n0[e] = canon(ror8m<0xC>(r0[e], r1[e])); n1[e] = canon(ror8m<0x3>(r1[e], r0[e])); }
    stc4(ws, o3, n0);
    stc4(ws, o3b, n1);
  }
  STAMP(10);
  if (P_WO == 0) WO_DMA();

  const u32 fb = (h >> 1) & 3, fm = j * 64 + (h & 1) * 32 + l7 * 4 + (lane >> 5) * 2 + ((lane >> 4) & 1);
  u32 osn = W_ST + h * 2048 + ((w >> 1) * 32 + l3) * 16 + (w & 1) * 256, oso = osn ^ 256;
  u32 od = W_H3 + ((h * 8 + j) * 128 + w * 16) * 128 + lane * 16;
  u32 icb = L_CB + l7 * 128 + ((((w & 1) * 4 + (w >> 1)) ^ l7) << 4) + (lane >> 5) * 8 + ((lane >> 4) & 1) * 4;
  u32 vu = L_Y + (g * 4 + tq) * 256 + (((2 * w + (tp >> 1)) ^ (((g & 1) * 4 + tq) << 1)) << 4) + (tp & 1) * 8;
  u32 rcb = L_CB + g * 128 + ((l7 ^ g) << 4);
  u32 o4s = W_H4 + h * 8192 + w * 1024 + j * 128 + bq * 32 + g * 8;
  PIN(osn); PIN(oso); PIN(od); PIN(icb); PIN(vu); PIN(rcb); PIN(o4s);
  if (ffn) {
    WAITV(P_WO == 0 ? 12 : 2);
    u32x4 fa[8];
#pragma unroll
    for (int t = 0; t < 4; ++t) {
      const u32 ta = L_W1 + trn + (((2 * t + (tp >> 1)) ^ trf) << 4);
      fa[2 * t] = ltr(ta, ta + 512);
      fa[2 * t + 1] = ltr(ta + 4096, ta + 4096 + 512);
    }
    FENCE();
    f32x4 d[4];
#pragma unroll
    for (int t = 0; t < 4; ++t) d[t] = mfma(fa[2 * t], xf0, f32x4{0, 0, 0, 0});
#pragma unroll
    for (int t = 0; t < 4; ++t) d[t] = mfma(fa[2 * t + 1], xf1, d[t]);
    stc4(ws, ohf, canon4(sel(u & 2, sel(u & 1, d[3], d[2]), sel(u & 1, d[1], d[0]))));
    nap<(P_D3 > 10 ? P_D3 - 10 : 0)>();
  } else {
    nap<P_D3>();
  }
  {
    u32x4 e0, e1, e2, e3, d0, d1;
    STAMP(11);
    for (;;) {
      ldc4<0>(e0, ws, osn); ldc4<128>(e1, ws, osn); ldc4<0>(e2, ws, oso); ldc4<128>(e3, ws, oso);
      WAITV(0);
      PIN(e0); PIN(e1); PIN(e2); PIN(e3);
      const u32 bad = (e0.y ^ tag) | (e0.w ^ tag) | (e1.y ^ tag) | (e1.w ^ tag) | (e2.y ^ tag) | (e2.w ^ tag) | (e3.y ^ tag) | (e3.w ^ tag);
      if (none(bad != 0) || ++spins > SPIN_MAX) break;
    }
    STAMP(17);
    nap<P_D3B>();
    ldc4<0>(d0, ws, od); ldc4<1024>(d1, ws, od);
    if (P_WO == 1) WO_DMA();
    float m = fmx(fmx(uf(e0.x), uf(e1.x)), fmx(uf(e2.x), uf(e3.x)));
    m = max32(max16(fmx(m, ror<8>(m))));
    float w0 = ex2(uf(e0.x) - m), w1 = ex2(uf(e1.x) - m);
    float l = w0 * uf(e0.z) + w1 * uf(e1.z) + ex2(uf(e2.x) - m) * uf(e2.z) + ex2(uf(e3.x) - m) * uf(e3.z);
    l += ror<8>(l);
    l = add16(l, l);
    l = add32(l, l);
    const float il = rcp(l);
    w0 *= il; w1 *= il;
    PIN(w0); PIN(w1);
    if (P_WO != 1) WAITV(0); else if (ffn) WAITV(10); else WAITV(2);
    for (;;) {
      PIN(d0); PIN(d1);
      if (none(umax(umax4(d0), umax4(d1)) == SENT) || ++spins > SPIN_MAX) break;
      ldc4<0>(d0, ws, od); ldc4<1024>(d1, ws, od);
      WAITV(0);
    }
    STAMP(12);
    if (P_WO == 2) WO_DMA();
    float acc[8];
#pragma unroll
    for (int e = 0; e < 4; ++e) {
      acc[2 * e] = w0 * lo(d0[e]) + w1 * lo(d1[e]);
      acc[2 * e + 1] = w0 * hi(d0[e]) + w1 * hi(d1[e]);
    }
    float c0, c1;
    red8(acc, c0, c1);
    u32 o = pk(c0, c1);
    PIN(o);
    if (!(lane & 8)) lw1(icb, o);
  }
  STAMP(13);
  BARRIER();
  {
    const u32x4 a0 = ltr(vu, vu + 4096), b0 = lr4(rcb), a1 = ltr(vu + 8192, vu + 8192 + 4096), b1 = lr4((rcb ^ 64) + 512);
    FENCE();
    const f32x4 c = mfma(a0, b0, f32x4{0, 0, 0, 0}) + mfma(a1, b1, f32x4{0, 0, 0, 0});
    u32x2 v = {canon(pk(c.x + ror<4>(c.x), c.y + ror<4>(c.y))), canon(pk(c.z + ror<4>(c.z), c.w + ror<4>(c.w)))};
    PIN(v);
    if (u == 0) stc2(ws, o4s, v);
  }
  STAMP(14);
  if (P_WO != 0) { WAITV(1); BARRIER(); }
  if (w < 2) {
    const u32 t0 = L_WO + trn + (((4 * w + (tp >> 1)) ^ trf) << 4), t1 = t0 ^ 32;
    u32 o4 = W_H4 + h * 8192 + (g >> 1) * 1024 + u * 128 + bq * 32 + (g & 1) * 16, o4b = o4 + 4096;
    u32 o5s = W_H5 + (j * 8 + bq * 2 + w) * 4096 + h * 128 + u * 64 + g * 16;
    u32x4 fa[8];
#pragma unroll
    for (int ks = 0; ks < 4; ++ks) { fa[ks] = ltr(t0 + ks * 4096, t0 + ks * 4096 + 512); fa[4 + ks] = ltr(t1 + ks * 4096, t1 + ks * 4096 + 512); }
#pragma unroll
    for (int i = 0; i < 8; ++i) PIN(fa[i]);
    PIN(o4); PIN(o4b); PIN(o5s);
    nap<P_D4>();
    u32x4 p[8];
    for (;;) {
      ldc4<0>(p[0], ws, o4); ldc4<512>(p[1], ws, o4); ldc4<2048>(p[2], ws, o4); ldc4<2560>(p[3], ws, o4);
      ldc4<0>(p[4], ws, o4b); ldc4<512>(p[5], ws, o4b); ldc4<2048>(p[6], ws, o4b); ldc4<2560>(p[7], ws, o4b);
      WAITV(0);
      u32 t = 0;
#pragma unroll
      for (int i = 0; i < 8; ++i) { PIN(p[i]); t = umax(t, umax4(p[i])); }
      if (none(t == SENT) || ++spins > SPIN_MAX) break;
    }
    STAMP(15);
    f32x4 c[4] = {{0, 0, 0, 0}, {0, 0, 0, 0}, {0, 0, 0, 0}, {0, 0, 0, 0}};
#pragma unroll
    for (int ks = 0; ks < 4; ++ks) {
      c[0] = mfma(fa[ks], p[2 * ks], c[0]);
      c[1] = mfma(fa[4 + ks], p[2 * ks], c[1]);
      c[2] = mfma(fa[ks], p[2 * ks + 1], c[2]);
      c[3] = mfma(fa[4 + ks], p[2 * ks + 1], c[3]);
    }
    f32x4 c0 = c[0] + c[2], c1 = c[1] + c[3];
#pragma unroll
    for (int e = 0; e < 4; ++e) {
      c0[e] += ror<8>(c0[e]); c1[e] += ror<8>(c1[e]);
      c0[e] += ror<4>(c0[e]); c1[e] += ror<4>(c1[e]);
    }
    u32x4 o = canon4(sel(u & 1, c1, c0));
    PIN(o);
    if (u < 2) stc4(ws, o5s, o);
  } else if (ffn) {
    const u32 of = W_HF + h * 8192 + lane * 16;
    const u32 f2 = (((g & 1) << 1) | (tq >> 1)) << 1;
    u32x4 p[8], fa[8];
    u32x2 b1v;
    ldg2(b1v, a.b1, (h * 64 + u * 16 + g * 4) * 2);
    for (;;) {
      ldc4<0>(p[0], ws, of); ldc4<1024>(p[1], ws, of); ldc4<2048>(p[2], ws, of); ldc4<3072>(p[3], ws, of);
      ldc4<0>(p[4], ws, of + 4096); ldc4<1024>(p[5], ws, of + 4096); ldc4<2048>(p[6], ws, of + 4096); ldc4<3072>(p[7], ws, of + 4096);
#pragma unroll
      for (int t = 0; t < 4; ++t) {
        const u32 ta = L_W2 + (g * 4 + tq) * 128 + (((2 * t + (tp >> 1)) ^ f2) << 4) + (tp & 1) * 8;
        fa[2 * t] = ltr(ta, ta + 2048);
        fa[2 * t + 1] = ltr(ta + 4096, ta + 4096 + 2048);
      }
      WAITV(0);
      u32 t = 0;
#pragma unroll
      for (int i = 0; i < 8; ++i) { PIN(p[i]); t = umax(t, umax4(p[i])); }
      if (none(t == SENT) || ++spins > SPIN_MAX) break;
    }
    PIN(b1v);
    const float bb[4] = {lo(b1v.x), hi(b1v.x), lo(b1v.y), hi(b1v.y)};
    float ge[4];
#pragma unroll
    for (int e = 0; e < 4; ++e) {
      const float s = ((uf(p[0][e]) + uf(p[1][e])) + (uf(p[2][e]) + uf(p[3][e]))) + ((uf(p[4][e]) + uf(p[5][e])) + (uf(p[6][e]) + uf(p[7][e])));
      const float t = rbf(rbf(s) + bb[e]);
      const float z = ex2(2.302208198f * (t + 0.044715f * t * t * t));
      ge[e] = t - t * rcp(z + 1.f);
    }
    lw2(L_HB + (u >> 1) * 256 + bfr + (u & 1) * 8, u32x2{pk(ge[0], ge[1]), pk(ge[2], ge[3])});
    asm volatile("s_waitcnt lgkmcnt(0)" ::: "memory");
    const u32x4 hb0 = lr4(L_HB + bfr), hb1 = lr4(L_HB + 256 + bfr);
    f32x4 d[4];
#pragma unroll
    for (int t = 0; t < 4; ++t) d[t] = mfma(fa[2 * t], hb0, f32x4{0, 0, 0, 0});
#pragma unroll
    for (int t = 0; t < 4; ++t) d[t] = mfma(fa[2 * t + 1], hb1, d[t]);
    stc4(ws, W_HY + o5, canon4(sel(u & 2, sel(u & 1, d[3], d[2]), sel(u & 1, d[1], d[0]))));
    STAMP(15);
  } else if (fin) {
    u32 fy = W_HY + (j * 8 + (h & 7)) * 4096 + lane * 16, fy3 = lane < 32 ? fy + 3072 : fy, fs = fy + (W_H5 - W_HY);
    u32 oo = (fb * 512 + fm) * 2;
    u32 xo, b2o;
    ldgh(xo, a.x, oo);
    ldgh(b2o, a.b2, fm * 2);
    PIN(fy); PIN(fy3); PIN(fs); PIN(oo);
    nap<P_D5>();
    u32x4 p0, p1, p2, p3;
    for (;;) {
      ldc4<0>(p0, ws, fy); ldc4<1024>(p1, ws, fy); ldc4<2048>(p2, ws, fy); ldc4<0>(p3, ws, fy3);
      WAITV(0);
      PIN(p0); PIN(p1); PIN(p2); PIN(p3);
      if (none(umax(umax(umax4(p0), umax4(p1)), umax(umax4(p2), umax4(p3))) == SENT) || ++spins > SPIN_MAX) break;
    }
    STAMP(15);
    u32x4 q0, q1, q2, q3;
    ldc4<0>(q0, ws, fs); ldc4<1024>(q1, ws, fs); ldc4<2048>(q2, ws, fs); ldc4<3072>(q3, ws, fs);
    PIN(xo); PIN(b2o);
    const f32x4 z = {0, 0, 0, 0};
    const f32x4 sy = __builtin_bit_cast(f32x4, p0) + __builtin_bit_cast(f32x4, p1) + __builtin_bit_cast(f32x4, p2) + sel(lane < 32, __builtin_bit_cast(f32x4, p3), z);
    const float y = rbf(rbf(red4(sy)) + lo(b2o));
    float gate = rbf(y * rcp(1.f + ex2(-1.4426950408889634f * y)));
    PIN(gate);
    WAITV(0);
    for (;;) {
      PIN(q0); PIN(q1); PIN(q2); PIN(q3);
      if (none(umax(umax(umax4(q0), umax4(q1)), umax(umax4(q2), umax4(q3))) == SENT) || ++spins > SPIN_MAX) break;
      ldc4<0>(q0, ws, fs); ldc4<1024>(q1, ws, fs); ldc4<2048>(q2, ws, fs); ldc4<3072>(q3, ws, fs);
      WAITV(0);
    }
    const f32x4 s = (__builtin_bit_cast(f32x4, q0) + __builtin_bit_cast(f32x4, q1)) + (__builtin_bit_cast(f32x4, q2) + __builtin_bit_cast(f32x4, q3));
    const float o = lo(xo) + rbf(rbf(red4(s)) * gate);
    u32 ov = pk(o, o);
    PIN(ov);
    if (!(lane & 8)) asm volatile("global_store_short %0, %1, %2 " OUTPOL ::"v"(oo), "v"(ov), "s"(a.out) : "memory");
    if (h == 24 && j == 0 && lane == 0) stc1(ws, 0, tag);
  }
  STAMP(18);
}

extern "C" void kernel_launch(void* const* d_in, const int* in_sizes, int n_in, void* d_out, int out_size, void* d_ws, size_t ws_size, hipStream_t stream) {
  Args a;
  a.x = (const char*)d_in[0];
  a.ckv = (const char*)d_in[1];
  a.wq = (const char*)d_in[2];
  a.wuk = (const char*)d_in[3];
  a.wuv = (const char*)d_in[4];
  a.wo = (const char*)d_in[5];
  a.w1 = (const char*)d_in[7];
  a.b1 = (const char*)d_in[8];
  a.w2 = (const char*)d_in[9];
  a.b2 = (const char*)d_in[10];
  a.out = (char*)d_out;
  a.ws = (char*)d_ws;
  (void)hipFuncSetAttribute((const void*)mla_ffn_decode, hipFuncAttributeMaxDynamicSharedMemorySize, L_TOTAL);
  mla_ffn_decode<<<256, 512, L_TOTAL, stream>>>(a);
}
```

```cpp
#define EXP 1
#include <hip/hip_runtime.h>
#include <hip/hip_bf16.h>
#include <stdint.h>

typedef unsigned u32;
typedef unsigned short u16;
typedef u32 u32x2 __attribute__((ext_vector_type(2)));
typedef u32 u32x4 __attribute__((ext_vector_type(4)));
typedef float f32x2 __attribute__((ext_vector_type(2)));
typedef float f32x4 __attribute__((ext_vector_type(4)));
typedef __bf16 bf16x2 __attribute__((ext_vector_type(2)));
typedef __bf16 bf16x8 __attribute__((ext_vector_type(8)));
typedef short s16x4 __attribute__((ext_vector_type(4)));
#define LAS __attribute__((address_space(3)))
#define DI __device__ __forceinline__

#define OUTPOL "sc1"
#define P_W 6
#define P_ST 1
#define P_OFF 0
#define P_DS 0
#define KVNT 0
#define P_D1 12
#define P_D2 14
#define P_D3 8
#define P_D4 12
#define P_D5 40
#define P_WO 1
#define P_UKL 1
#define P_D3B 0
constexpr u32 SPIN_MAX = 1u << 16;
constexpr u32 SENT = 0xFFFFFFFFu;

constexpr u32 W_H1 = 0x1000, W_H2 = W_H1 + 0x80000, W_ST = W_H2 + 0x40000, W_HF = W_ST + 0x10000;
constexpr u32 W_H4 = W_HF + 0x40000, W_H5 = W_H4 + 0x40000, W_HY = W_H5 + 0x40000, W_H3 = W_HY + 0x40000;
constexpr u32 W_DG = W_H3 + 0x400000;

constexpr u32 L_X = 131072, L_Y = L_X + 16384, L_TOTAL = L_Y + 16384;
constexpr u32 L_Q0 = 31 * 1024, L_XN = 63 * 1024, L_FL = 95 * 1024;
constexpr u32 L_W1 = L_X, L_MX = L_X + 8192, L_LS = L_X + 8704, L_P = L_X + 9216, L_CB = L_X + 13312, L_CX = L_X + 14336, L_HB = L_X + 15360;
constexpr u32 L_WO = 0, L_W2 = 16384;

struct Args {
  const char *x, *ckv, *wq, *wuk, *wuv, *wo, *w1, *b1, *w2, *b2;
  char *out, *ws;
};

#define WAITV(n) asm volatile("s_waitcnt vmcnt(%0)" ::"i"(n) : "memory")
#define BARRIER() asm volatile("s_waitcnt lgkmcnt(0)\n\ts_barrier" ::: "memory")
#define PIN(v) asm volatile("" : "+v"(v))
#define FENCE() __builtin_amdgcn_sched_barrier(0)

template <int NT> DI void dma(const char* base, u32 voff, u32 lds) {
  u32 keep;
  lds = __builtin_amdgcn_readfirstlane(lds);
  if (NT)
    asm volatile("s_mov_b32 %0, m0\n\ts_mov_b32 m0, %3\n\ts_nop 0\n\tglobal_load_lds_dwordx4 %1, %2 nt\n\ts_mov_b32 m0, %0"
                 : "=&s"(keep) : "v"(voff), "s"(base), "s"(lds) : "memory");
  else
    asm volatile("s_mov_b32 %0, m0\n\ts_mov_b32 m0, %3\n\ts_nop 0\n\tglobal_load_lds_dwordx4 %1, %2\n\ts_mov_b32 m0, %0"
                 : "=&s"(keep) : "v"(voff), "s"(base), "s"(lds) : "memory");
}
template <int OFF> DI void ldc4(u32x4& d, const char* base, u32 voff) {
  asm volatile("global_load_dwordx4 %0, %1, %2 offset:%3 sc1" : "=&v"(d) : "v"(voff), "s"(base), "i"(OFF) : "memory");
}
template <int OFF> DI void ldc2(u32x2& d, const char* base, u32 voff) {
  asm volatile("global_load_dwordx2 %0, %1, %2 offset:%3 sc1" : "=&v"(d) : "v"(voff), "s"(base), "i"(OFF) : "memory");
}
DI void ldg4(u32x4& d, const char* base, u32 voff) {
  asm volatile("global_load_dwordx4 %0, %1, %2" : "=&v"(d) : "v"(voff), "s"(base) : "memory");
}
DI void ldg2(u32x2& d, const char* base, u32 voff) {
  asm volatile("global_load_dwordx2 %0, %1, %2" : "=&v"(d) : "v"(voff), "s"(base) : "memory");
}
DI void ldgh(u32& d, const char* base, u32 voff) {
  asm volatile("global_load_ushort %0, %1, %2" : "=&v"(d) : "v"(voff), "s"(base) : "memory");
}
DI void stc4(const char* base, u32 voff, u32x4 v) {
  asm volatile("global_store_dwordx4 %0, %1, %2 sc1\n\ts_nop 1" ::"v"(voff), "v"(v), "s"(base) : "memory");
}
DI void stc2(const char* base, u32 voff, u32x2 v) {
  asm volatile("global_store_dwordx2 %0, %1, %2 sc1" ::"v"(voff), "v"(v), "s"(base) : "memory");
}
DI void stc1(const char* base, u32 voff, u32 v) {
  asm volatile("global_store_dword %0, %1, %2 sc1" ::"v"(voff), "v"(v), "s"(base) : "memory");
}

DI u32x4 lr4(u32 a) { return *(LAS u32x4*)a; }
DI void lw4(u32 a, u32x4 v) { *(LAS u32x4*)a = v; }
DI void lw2(u32 a, u32x2 v) { *(LAS u32x2*)a = v; }
DI void lw1(u32 a, u32 v) { *(LAS u32*)a = v; }
DI u32x4 ltr(u32 a0, u32 a1) {
  u32x2 p = __builtin_bit_cast(u32x2, __builtin_amdgcn_ds_read_tr16_b64_v4i16((LAS s16x4*)a0));
  u32x2 q = __builtin_bit_cast(u32x2, __builtin_amdgcn_ds_read_tr16_b64_v4i16((LAS s16x4*)a1));
  return u32x4{p.x, p.y, q.x, q.y};
}
DI f32x4 mfma(u32x4 a, u32x4 b, f32x4 c) {
  return __builtin_amdgcn_mfma_f32_16x16x32_bf16(__builtin_bit_cast(bf16x8, a), __builtin_bit_cast(bf16x8, b), c, 0, 0, 0);
}
DI u32 fu(float f) { return __float_as_uint(f); }
DI float uf(u32 u) { return __uint_as_float(u); }
DI u32 pk(float a, float b) { return __builtin_bit_cast(u32, __builtin_convertvector(f32x2{a, b}, bf16x2)); }
DI float lo(u32 u) { return uf(u << 16); }
DI float hi(u32 u) { return uf(u & 0xffff0000u); }
DI float rbf(float a) { return lo(pk(a, a)); }
DI u32 canon(u32 u) { return u < SENT - 1 ? u : SENT - 1; }
DI u32x4 canon4(f32x4 v) { return u32x4{canon(fu(v.x)), canon(fu(v.y)), canon(fu(v.z)), canon(fu(v.w))}; }
#pragma float_control(push)
#pragma float_control(precise, off)
DI float fmx(float a, float b) { return __builtin_fmaxf(a, b); }
#pragma float_control(pop)
DI u32 umax(u32 a, u32 b) { return a > b ? a : b; }
DI u32 umax4(u32x4 v) { return umax(umax(v.x, v.y), umax(v.z, v.w)); }
template <int C> DI float ror(float v) { return uf((u32)__builtin_amdgcn_update_dpp(0, (int)fu(v), 0x120 + C, 0xf, 0xf, true)); }
template <int BANKS> DI u32 ror8m(u32 keep, u32 src) { return (u32)__builtin_amdgcn_update_dpp((int)keep, (int)src, 0x128, 0xf, BANKS, false); }
DI float add32(float a, float b) { auto s = __builtin_amdgcn_permlane32_swap(fu(a), fu(b), false, false); return uf(s[0]) + uf(s[1]); }
DI float add16(float a, float b) { auto s = __builtin_amdgcn_permlane16_swap(fu(a), fu(b), false, false); return uf(s[0]) + uf(s[1]); }
DI float max32(float a) { auto s = __builtin_amdgcn_permlane32_swap(fu(a), fu(a), false, false); return fmx(uf(s[0]), uf(s[1])); }
DI float max16(float a) { auto s = __builtin_amdgcn_permlane16_swap(fu(a), fu(a), false, false); return fmx(uf(s[0]), uf(s[1])); }
DI float wsum(float v) {
  v += ror<8>(v); v += ror<4>(v); v += ror<2>(v); v += ror<1>(v);
  v = add16(v, v);
  return add32(v, v);
}
DI f32x4 sel(bool c, f32x4 a, f32x4 b) { return f32x4{c ? a.x : b.x, c ? a.y : b.y, c ? a.z : b.z, c ? a.w : b.w}; }
DI void lds_signal(u32 a) {
  asm volatile("s_waitcnt lgkmcnt(0)" ::: "memory");
  lw1(a, 1u);
}
DI void lds_wait4(u32 a, u32& spins) {
  for (;;) {
    asm volatile("" ::: "memory");
    const u32x4 f = lr4(a);
    if (__builtin_amdgcn_readfirstlane(f.x & f.y & f.z & f.w) || ++spins > SPIN_MAX) break;
  }
  asm volatile("" ::: "memory");
}
DI bool none(bool bad) { return __builtin_amdgcn_ballot_w64(bad) == 0; }
DI float ex2(float v) { return __builtin_amdgcn_exp2f(v); }
DI float rcp(float v) { return __builtin_amdgcn_rcpf(v); }
template <int N> DI void nap() { if (N > 0) __builtin_amdgcn_s_sleep(N); }

DI void red8(const float (&a)[8], float& o0, float& o1) {
  float r0 = add32(a[0], a[4]), r1 = add32(a[1], a[5]), r2 = add32(a[2], a[6]), r3 = add32(a[3], a[7]);
  float s0 = add16(r0, r2), s1 = add16(r1, r3);
  o0 = s0 + ror<8>(s0);
  o1 = s1 + ror<8>(s1);
}
DI float red4(f32x4 a) {
  float r0 = add32(a.x, a.z), r1 = add32(a.y, a.w);
  float s = add16(r0, r1);
  return s + ror<8>(s);
}

#define STAMP(i)
#define STAMPW(i)

__global__ __launch_bounds__(512, 2) void mla_ffn_decode(Args a) {
  const u32 tid = threadIdx.x, bid = blockIdx.x, lane = tid & 63;
  const u32 w = __builtin_amdgcn_readfirstlane(tid >> 6);
  const u32 at0 = (u32)__builtin_amdgcn_s_memrealtime();
  const u32 xcd = bid & 7, r = bid >> 3, grp = r & 1, j = (r >> 1) & 7, h = grp * 16 + xcd * 2 + (r >> 4);
  const u32 ab = xcd >> 1, aks = (xcd & 1) * 16 + (r >> 1), hg = 1 - grp, sp = ab * 32 + aks;
  const u32 mq = j >> 1, dh = j & 1;
  const u32 l15 = lane & 15, g = lane >> 4, bq = lane & 3, u = (lane >> 2) & 3, l7 = lane & 7, l3 = lane >> 3;
  const u32 tq = u, tp = bq;
  const bool ffn = w == 3 && h < 28, fin = w == 7 && h >= 24;
  if (w == 0) lw2(L_FL + lane * 8, u32x2{0, 0});
  BARRIER();
  const char* ws = a.ws;
  const u32 tag = *(const __attribute__((address_space(4))) u32*)ws + 1;
  u32 spins = 0;

  const u32 f1 = (l3 >> 1) & 1;
  const u32 trn = (g * 8 + tq) * 128 + (tp & 1) * 8, trf = ((tq >> 1) | ((g & 1) << 1)) << 1;
  const u32x4 sent4 = {SENT, SENT, SENT, SENT};
  u32 o5, ohf, ost, o3, o3b, bfr, va, frag[4];
  u32x4 xf0, xf1;
#define COMMON_ADDR()                                                                                        \
  do {                                                                                                       \
    o5 = (j * 8 + bq * 2 + (u >> 1)) * 4096 + h * 128 + (u & 1) * 64 + g * 16;                               \
    ohf = W_HF + h * 8192 + j * 1024 + lane * 16;                                                            \
    ost = W_ST + (hg * 16 + l15) * 2048 + sp * 16;                                                           \
    o3 = W_H3 + (((hg * 16 + l7) * 8 + w) * 128 + sp) * 128 + (((lane >> 3) & 1) * 4 + g) * 16;              \
    o3b = o3 + 8 * 8 * 16384;                                                                                \
    bfr = g * 64 + bq * 16;                                                                                  \
    va = (g * 4 + tq) * 1024 + (((w * 8 + (tp >> 1)) ^ (((g & 1) * 4 + tq) << 1)) << 4) + (tp & 1) * 8;      \
    _Pragma("unroll") for (int i = 0; i < 4; ++i) frag[i] = (l15 * 1024 + ((g ^ (l7 << 1)) << 4)) ^ (i << 6); \
    PIN(o5); PIN(ohf); PIN(ost); PIN(o3); PIN(o3b); PIN(bfr); PIN(va);                                       \
    PIN(frag[0]); PIN(frag[1]); PIN(frag[2]); PIN(frag[3]);                                                  \
  } while (0)
  const char* kvb = a.ckv + (size_t)(ab * 4096 + aks * 128 + (w & 3) * 32) * 1024;

  if (w < 4) {
    u32x4 xv;
    ldg4(xv, a.x, w * 1024 + lane * 16);
    const u32 vq = ((mq * 128 + w * 32 + l3) * 32 + h) * 256 + dh * 128;
    const u32 vq0 = vq + ((l7 ^ (f1 << 1)) << 4), vq1 = vq + 8 * 8192 + ((l7 ^ ((f1 | 2) << 1)) << 4);
    dma<1>(a.wq, vq0, L_X + w * 4096);
    dma<1>(a.wq, vq1, L_X + w * 4096 + 1024);
    dma<1>(a.wq + 16 * 8192, vq0, L_X + w * 4096 + 2048);
    dma<1>(a.wq + 16 * 8192, vq1, L_X + w * 4096 + 3072);
#define UK_DMA()                                                                                              \
  _Pragma("unroll") for (int i = 0; i < 4; ++i)                                                               \
      dma<1>(a.wuk + i * 4 * 8192, ((j * 64 + w * 16 + g) * 32 + h) * 256 + ((l15 ^ (i * 4 + g)) << 4), L_Y + w * 4096 + i * 1024)
    if (!P_UKL) { UK_DMA(); }
    COMMON_ADDR();
    u32 oc = w == 0 ? ohf : w == 1 ? W_H4 + h * 8192 + l3 * 1024 + j * 128 + l7 * 16 : w == 2 ? W_H5 + o5 : W_HY + o5;
    u32 taq = L_X + trn + (((2 * w + (tp >> 1)) ^ trf) << 4);
    u32 o1s = W_H1 + h * 16384 + dh * 8192 + w * 2048 + mq * 512 + (g * 8 + l7) * 16;
    u32 o1 = W_H1 + h * 16384 + w * 4096 + lane * 16;
    u32 iq = L_Q0 + (4 * w + (lane >> 5) * 2 + (g & 1)) * 64 + bq * 16 + (l3 & 1) * 8 + (u & 1) * 4;
    u32 ua = L_Y + (w * 32 + l15) * 256 + ((g ^ l15) << 4);
    u32 o2s = W_H2 + (bq * 32 + h) * 2048 + j * 256 + w * 128 + u * 64 + g * 16;
    u32 o2 = W_H2 + (ab * 32 + hg * 16 + 4 * w) * 2048 + lane * 16, o2b = o2 + 4096;
    u32 i0 = L_X + 4 * w * 1024 + (((lane >> 1) ^ (((4 * w) & 7) << 1)) << 4) + (lane & 1) * 8;
    PIN(oc); PIN(taq); PIN(o1s); PIN(o1); PIN(iq); PIN(ua); PIN(o2s); PIN(o2); PIN(o2b); PIN(i0);

    WAITV(P_UKL ? 4 : 8);
    STAMP(19);
    PIN(xv);
    {
      float v[8] = {lo(xv.x), hi(xv.x), lo(xv.y), hi(xv.y), lo(xv.z), hi(xv.z), lo(xv.w), hi(xv.w)};
      const float mean = wsum(((v[0] + v[1]) + (v[2] + v[3])) + ((v[4] + v[5]) + (v[6] + v[7]))) * (1.f / 512.f);
      float d[8];
#pragma unroll
      for (int i = 0; i < 8; ++i) { d[i] = v[i] - mean; d[i] *= d[i]; }
      const float var = rbf(wsum(((d[0] + d[1]) + (d[2] + d[3])) + ((d[4] + d[5]) + (d[6] + d[7]))) * (1.f / 512.f)), mb = rbf(mean);
      const float inv = rcp(rbf(__builtin_amdgcn_sqrtf(rbf(var + 1e-5f))));
      u32x4 o;
#pragma unroll
      for (int i = 0; i < 4; ++i) { const u32 c = pk(v[2 * i] - mb, v[2 * i + 1] - mb); o[i] = pk(lo(c) * inv, hi(c) * inv); }
      PIN(o);
      if (g == mq) lw4(L_XN + l15 * 64 + w * 16, o);
    }
    WAITV(P_UKL ? 0 : 4);
    STAMP(1);
    lds_signal(L_FL + w * 4);
    if (P_UKL == 1) { UK_DMA(); }
    lds_wait4(L_FL, spins);
    {
      u32x4 fa[4], fb[4];
#pragma unroll
      for (int ks = 0; ks < 4; ++ks) { fa[ks] = ltr(taq + ks * 4096, taq + ks * 4096 + 512); fb[ks] = lr4(L_XN + ks * 256 + bfr); }
      FENCE();
      f32x4 c0 = mfma(fa[0], fb[0], f32x4{0, 0, 0, 0}), c1 = mfma(fa[1], fb[1], f32x4{0, 0, 0, 0});
      c0 = mfma(fa[2], fb[2], c0);
      c1 = mfma(fa[3], fb[3], c1);
      xf0 = dh ? fb[2] : fb[0];
      xf1 = dh ? fb[3] : fb[1];
      const f32x4 acc = c0 + c1;
      u32x4 o = {fu((u & 1) ? acc.z : acc.x), tag, fu((u & 1) ? acc.w : acc.y), tag};
      PIN(o);
      if (u < 2) stc4(ws, o1s, o);
    }
    STAMP(2);
    if (P_ST == 1) lw1(L_FL + 32 + w * 4, 1u);
    if (P_UKL == 2) { UK_DMA(); }
    stc4(ws, oc, sent4);
    {
      u32x4 p0, p1, p2, p3;
      nap<P_D1>();
      for (;;) {
        ldc4<0>(p0, ws, o1); ldc4<1024>(p1, ws, o1); ldc4<2048>(p2, ws, o1); ldc4<3072>(p3, ws, o1);
        if (P_ST == 2) lw1(L_FL + 32 + w * 4, 1u);
        WAITV(0);
        PIN(p0); PIN(p1); PIN(p2); PIN(p3);
        const u32 bad = (p0.y ^ tag) | (p0.w ^ tag) | (p1.y ^ tag) | (p1.w ^ tag) | (p2.y ^ tag) | (p2.w ^ tag) | (p3.y ^ tag) | (p3.w ^ tag);
        if (none(bad != 0) || ++spins > SPIN_MAX) break;
      }
      lw1(iq, pk(add32(uf(p0.x) + uf(p1.x), uf(p2.x) + uf(p3.x)), add32(uf(p0.z) + uf(p1.z), uf(p2.z) + uf(p3.z))));
    }
    STAMP(3);
    if (P_ST == 3) lw1(L_FL + 32 + w * 4, 1u);
    lds_signal(L_FL + 16 + w * 4);
    if (w < 2) {
      u32x4 fb[4], f0[4], f1v[4];
      lds_wait4(L_FL + 16, spins);
#pragma unroll
      for (int ks = 0; ks < 4; ++ks) { fb[ks] = lr4(L_Q0 + bfr + ks * 256); f0[ks] = lr4(ua ^ (ks << 6)); f1v[ks] = lr4((ua ^ (ks << 6)) + 4096); }
      FENCE();
      f32x4 c0 = {0, 0, 0, 0}, c1 = {0, 0, 0, 0};
#pragma unroll
      for (int ks = 0; ks < 4; ++ks) { c0 = mfma(f0[ks], fb[ks], c0); c1 = mfma(f1v[ks], fb[ks], c1); }
      const float sc = 0.08838834764831845f * 1.4426950408889634f;
      const f32x4 c = sel(u & 1, c1, c0) * sc;
      u32x4 o = {pk(c.x, c.y), tag, pk(c.z, c.w), tag};
      PIN(o);
      if (u < 2) stc4(ws, o2s, o);
      STAMP(4);
      nap<P_D2>();
    } else {
      lds_wait4(L_FL + 16, spins);
      nap<P_D2 + 6>();
    }
    u32x4 s[8];
    for (;;) {
      ldc4<0>(s[0], ws, o2); ldc4<1024>(s[1], ws, o2); ldc4<2048>(s[2], ws, o2); ldc4<3072>(s[3], ws, o2);
      ldc4<0>(s[4], ws, o2b); ldc4<1024>(s[5], ws, o2b); ldc4<2048>(s[6], ws, o2b); ldc4<3072>(s[7], ws, o2b);
      WAITV(0);
      u32 bad = 0;
#pragma unroll
      for (int i = 0; i < 8; ++i) { PIN(s[i]); bad |= (s[i].y ^ tag) | (s[i].w ^ tag); }
      if (none(bad != 0) || ++spins > SPIN_MAX) break;
    }
#pragma unroll
    for (int i = 0; i < 8; ++i) lw2((i0 ^ ((i >> 1) << 5)) + (i >> 1) * 1024 + (i & 1) * 512, u32x2{s[i].x, s[i].z});
    STAMP(5);
    BARRIER();
  } else {
    COMMON_ADDR();
    if (P_ST) lds_wait4(L_FL + 32, spins);
    nap<P_DS>();
    STAMP(2);
    const u32 lb = (w & 3) * 32768, off = grp ? P_OFF : 0;
#pragma unroll
    for (int i = 0; i < 31; ++i) {
      if (i >= P_W) WAITV(P_W - 1);
      const u32 row = i < 31 - P_OFF ? i + off : i - (31 - P_OFF) + (grp ? 0 : 31 - P_OFF);
      dma<KVNT>(kvb + row * 1024, (lane ^ ((row & 7) << 1)) << 4, lb + row * 1024);
    }
    STAMP(4);
    BARRIER();
    dma<KVNT>(kvb + 31 * 1024, (lane ^ 14) << 4, lb + 31 * 1024);
  }
  stc4(ws, o3, sent4);
  stc4(ws, o3b, sent4);
  const u32 lkv = w * 16384;
  const u32 vuk = ((j * 64 + w * 8 + g) * 32 + h) * 256;
  dma<1>(a.wuv, vuk + ((l15 ^ (g << 1)) << 4), L_Y + w * 2048);
  dma<1>(a.wuv, vuk + 4 * 8192 + ((l15 ^ ((4 + g) << 1)) << 4), L_Y + w * 2048 + 1024);

  u32x4 fr[16];
#pragma unroll
  for (int ks = 0; ks < 16; ++ks) fr[ks] = lr4(L_X + frag[ks & 3] + (ks >> 2) * 256);
#pragma unroll
  for (int i = 0; i < 4; ++i) { frag[i] += lkv; PIN(frag[i]); }
  STAMP(6);
  BARRIER();
  if (ffn) {
#pragma unroll
    for (int i = 0; i < 8; ++i)
      dma<1>(a.w1 + i * 8 * 3584, (j * 64 + l3) * 3584 + h * 128 + ((l7 ^ ((f1 | ((i & 1) << 1)) << 1)) << 4), L_W1 + i * 1024);
  }
  u32 imx = L_MX + l15 * 32, ip = L_P + lane * 16;
  PIN(imx); PIN(ip);
  if (w >= 4) WAITV(4);
  STAMP(7);
  BARRIER();

  f32x4 sv;
  {
    u32x4 fa[16];
#pragma unroll
    for (int ks = 0; ks < 16; ++ks) fa[ks] = lr4(frag[ks & 3] + (ks >> 2) * 256);
    FENCE();
    f32x4 c0 = {0, 0, 0, 0}, c1 = {0, 0, 0, 0};
#pragma unroll
    for (int ks = 0; ks < 16; ks += 2) { c0 = mfma(fa[ks], fr[ks], c0); c1 = mfma(fa[ks + 1], fr[ks + 1], c1); }
    sv = c0 + c1;
    float m = max32(max16(fmx(fmx(sv.x, sv.y), fmx(sv.z, sv.w))));
    PIN(m);
    if (g == 0) lw1(imx + w * 4, fu(m));
  }
  STAMP(8);
  BARRIER();
  float mx;
  {
    const u32x4 m0 = lr4(imx), m1 = lr4(imx + 16);
    FENCE();
#pragma unroll
    for (int ks = 0; ks < 4; ++ks)
#pragma unroll
      for (int t = 0; t < 4; ++t) fr[t * 4 + ks] = ltr((va ^ (t << 5)) + ks * 32768, (va ^ (t << 5)) + ks * 32768 + 16384);
    FENCE();
    mx = fmx(fmx(fmx(uf(m0.x), uf(m0.y)), fmx(uf(m0.z), uf(m0.w))), fmx(fmx(uf(m1.x), uf(m1.y)), fmx(uf(m1.z), uf(m1.w))));
    const u32 p0 = pk(ex2(sv.x - mx), ex2(sv.y - mx)), p1 = pk(ex2(sv.z - mx), ex2(sv.w - mx));
    lw2(ip + (w >> 1) * 1024 + (w & 1) * 8, u32x2{p0, p1});
    float l = (lo(p0) + hi(p0)) + (lo(p1) + hi(p1));
    l = add16(l, l);
    l = add32(l, l);
    PIN(l);
    if (g == 0) lw1(imx + (L_LS - L_MX) + w * 4, fu(l));
    if (ffn) WAITV(10); else WAITV(2);
  }
  STAMP(9);
  BARRIER();
#define WO_DMA()                                                                          \
  do {                                                                                    \
    if (ffn) {                                                                            \
      _Pragma("unroll") for (int i = 0; i < 8; ++i)                                       \
          dma<1>(a.w2 + i * 8192, (h * 64 + l3) * 1024 + j * 128 + ((l7 ^ (l3 & 6)) << 4), L_W2 + i * 1024); \
    }                                                                                     \
    const u32 vo = (h * 128 + w * 16 + l3) * 1024 + j * 128;                              \
    dma<1>(a.wo, vo + ((l7 ^ (f1 << 1)) << 4), L_WO + w * 2048);                          \
    dma<1>(a.wo, vo + 8 * 1024 + ((l7 ^ ((f1 | 2) << 1)) << 4), L_WO + w * 2048 + 1024);  \
  } while (0)
  {
    u32x4 pf[4];
#pragma unroll
    for (int ks = 0; ks < 4; ++ks) pf[ks] = lr4(ip + ks * 1024);
    if (w == 7) {
      const u32x4 l0 = lr4(imx + (L_LS - L_MX)), l1 = lr4(imx + (L_LS - L_MX) + 16);
      u32x4 o = {fu(mx), tag, fu(((uf(l0.x) + uf(l0.y)) + (uf(l0.z) + uf(l0.w))) + ((uf(l1.x) + uf(l1.y)) + (uf(l1.z) + uf(l1.w)))), tag};
      PIN(o);
      if (lane < 16) stc4(ws, ost, o);
    }
    FENCE();
    f32x4 o[4] = {{0, 0, 0, 0}, {0, 0, 0, 0}, {0, 0, 0, 0}, {0, 0, 0, 0}};
#pragma unroll
    for (int ks = 0; ks < 4; ++ks)
#pragma unroll
      for (int t = 0; t < 4; ++t) o[t] = mfma(fr[t * 4 + ks], pf[ks], o[t]);
    const u32x4 r0 = {pk(o[0].x, o[0].y), pk(o[0].z, o[0].w), pk(o[1].x, o[1].y), pk(o[1].z, o[1].w)};
    const u32x4 r1 = {pk(o[2].x, o[2].y), pk(o[2].z, o[2].w), pk(o[3].x, o[3].y), pk(o[3].z, o[3].w)};
    u32x4 n0, n1;
#pragma unroll
    for (int e = 0; e < 4; ++e) { n0[e] = canon(ror8m<0xC>(r0[e], r1[e])); n1[e] = canon(ror8m<0x3>(r1[e], r0[e])); }
    stc4(ws, o3, n0);
    stc4(ws, o3b, n1);
  }
  STAMP(10);
  if (P_WO == 0) WO_DMA();

  const u32 fb = (h >> 1) & 3, fm = j * 64 + (h & 1) * 32 + l7 * 4 + (lane >> 5) * 2 + ((lane >> 4) & 1);
  u32 osn = W_ST + h * 2048 + ((w >> 1) * 32 + l3) * 16 + (w & 1) * 256, oso = osn ^ 256;
  u32 od = W_H3 + ((h * 8 + j) * 128 + w * 16) * 128 + lane * 16;
  u32 icb = L_CB + l7 * 128 + ((((w & 1) * 4 + (w >> 1)) ^ l7) << 4) + (lane >> 5) * 8 + ((lane >> 4) & 1) * 4;
  u32 vu = L_Y + (g * 4 + tq) * 256 + (((2 * w + (tp >> 1)) ^ (((g & 1) * 4 + tq) << 1)) << 4) + (tp & 1) * 8;
  u32 rcb = L_CB + g * 128 + ((l7 ^ g) << 4);
  u32 o4s = W_H4 + h * 8192 + w * 1024 + j * 128 + bq * 32 + g * 8;
  PIN(osn); PIN(oso); PIN(od); PIN(icb); PIN(vu); PIN(rcb); PIN(o4s);
  if (ffn) {
    WAITV(P_WO == 0 ? 12 : 2);
    u32x4 fa[8];
#pragma unroll
    for (int t = 0; t < 4; ++t) {
      const u32 ta = L_W1 + trn + (((2 * t + (tp >> 1)) ^ trf) << 4);
      fa[2 * t] = ltr(ta, ta + 512);
      fa[2 * t + 1] = ltr(ta + 4096, ta + 4096 + 512);
    }
    FENCE();
    f32x4 d[4];
#pragma unroll
    for (int t = 0; t < 4; ++t) d[t] = mfma(fa[2 * t], xf0, f32x4{0, 0, 0, 0});
#pragma unroll
    for (int t = 0; t < 4; ++t) d[t] = mfma(fa[2 * t + 1], xf1, d[t]);
    stc4(ws, ohf, canon4(sel(u & 2, sel(u & 1, d[3], d[2]), sel(u & 1, d[1], d[0]))));
    nap<(P_D3 > 10 ? P_D3 - 10 : 0)>();
  } else {
    nap<P_D3>();
  }
  {
    u32x4 e0, e1, e2, e3, d0, d1;
    STAMP(11);
    for (;;) {
      ldc4<0>(e0, ws, osn); ldc4<128>(e1, ws, osn); ldc4<0>(e2, ws, oso); ldc4<128>(e3, ws, oso);
      WAITV(0);
      PIN(e0); PIN(e1); PIN(e2); PIN(e3);
      const u32 bad = (e0.y ^ tag) | (e0.w ^ tag) | (e1.y ^ tag) | (e1.w ^ tag) | (e2.y ^ tag) | (e2.w ^ tag) | (e3.y ^ tag) | (e3.w ^ tag);
      if (none(bad != 0) || ++spins > SPIN_MAX) break;
    }
    STAMP(17);
    nap<P_D3B>();
    ldc4<0>(d0, ws, od); ldc4<1024>(d1, ws, od);
    if (P_WO == 1) WO_DMA();
    float m = fmx(fmx(uf(e0.x), uf(e1.x)), fmx(uf(e2.x), uf(e3.x)));
    m = max32(max16(fmx(m, ror<8>(m))));
    float w0 = ex2(uf(e0.x) - m), w1 = ex2(uf(e1.x) - m);
    float l = w0 * uf(e0.z) + w1 * uf(e1.z) + ex2(uf(e2.x) - m) * uf(e2.z) + ex2(uf(e3.x) - m) * uf(e3.z);
    l += ror<8>(l);
    l = add16(l, l);
    l = add32(l, l);
    const float il = rcp(l);
    w0 *= il; w1 *= il;
    PIN(w0); PIN(w1);
    if (P_WO != 1) WAITV(0); else if (ffn) WAITV(10); else WAITV(2);
    for (;;) {
      PIN(d0); PIN(d1);
      if (none(umax(umax4(d0), umax4(d1)) == SENT) || ++spins > SPIN_MAX) break;
      ldc4<0>(d0, ws, od); ldc4<1024>(d1, ws, od);
      WAITV(0);
    }
    STAMP(12);
    if (P_WO == 2) WO_DMA();
    float acc[8];
#pragma unroll
    for (int e = 0; e < 4; ++e) {
      acc[2 * e] = w0 * lo(d0[e]) + w1 * lo(d1[e]);
      acc[2 * e + 1] = w0 * hi(d0[e]) + w1 * hi(d1[e]);
    }
    float c0, c1;
    red8(acc, c0, c1);
    u32 o = pk(c0, c1);
    PIN(o);
    if (!(lane & 8)) lw1(icb, o);
  }
  STAMP(13);
  BARRIER();
  {
    const u32x4 a0 = ltr(vu, vu + 4096), b0 = lr4(rcb), a1 = ltr(vu + 8192, vu + 8192 + 4096), b1 = lr4((rcb ^ 64) + 512);
    FENCE();
    const f32x4 c = mfma(a0, b0, f32x4{0, 0, 0, 0}) + mfma(a1, b1, f32x4{0, 0, 0, 0});
    u32x2 v = {canon(pk(c.x + ror<4>(c.x), c.y + ror<4>(c.y))), canon(pk(c.z + ror<4>(c.z), c.w + ror<4>(c.w)))};
    PIN(v);
    if (u == 0) stc2(ws, o4s, v);
  }
  STAMP(14);
  if (P_WO != 0) { WAITV(1); BARRIER(); }
  if (w < 2) {
    const u32 t0 = L_WO + trn + (((4 * w + (tp >> 1)) ^ trf) << 4), t1 = t0 ^ 32;
    u32 o4 = W_H4 + h * 8192 + (g >> 1) * 1024 + u * 128 + bq * 32 + (g & 1) * 16, o4b = o4 + 4096;
    u32 o5s = W_H5 + (j * 8 + bq * 2 + w) * 4096 + h * 128 + u * 64 + g * 16;
    u32x4 fa[8];
#pragma unroll
    for (int ks = 0; ks < 4; ++ks) { fa[ks] = ltr(t0 + ks * 4096, t0 + ks * 4096 + 512); fa[4 + ks] = ltr(t1 + ks * 4096, t1 + ks * 4096 + 512); }
#pragma unroll
    for (int i = 0; i < 8; ++i) PIN(fa[i]);
    PIN(o4); PIN(o4b); PIN(o5s);
    nap<P_D4>();
    u32x4 p[8];
    for (;;) {
      ldc4<0>(p[0], ws, o4); ldc4<512>(p[1], ws, o4); ldc4<2048>(p[2], ws, o4); ldc4<2560>(p[3], ws, o4);
      ldc4<0>(p[4], ws, o4b); ldc4<512>(p[5], ws, o4b); ldc4<2048>(p[6], ws, o4b); ldc4<2560>(p[7], ws, o4b);
      WAITV(0);
      u32 t = 0;
#pragma unroll
      for (int i = 0; i < 8; ++i) { PIN(p[i]); t = umax(t, umax4(p[i])); }
      if (none(t == SENT) || ++spins > SPIN_MAX) break;
    }
    STAMP(15);
    f32x4 c[4] = {{0, 0, 0, 0}, {0, 0, 0, 0}, {0, 0, 0, 0}, {0, 0, 0, 0}};
#pragma unroll
    for (int ks = 0; ks < 4; ++ks) {
      c[0] = mfma(fa[ks], p[2 * ks], c[0]);
      c[1] = mfma(fa[4 + ks], p[2 * ks], c[1]);
      c[2] = mfma(fa[ks], p[2 * ks + 1], c[2]);
      c[3] = mfma(fa[4 + ks], p[2 * ks + 1], c[3]);
    }
    f32x4 c0 = c[0] + c[2], c1 = c[1] + c[3];
#pragma unroll
    for (int e = 0; e < 4; ++e) {
      c0[e] += ror<8>(c0[e]); c1[e] += ror<8>(c1[e]);
      c0[e] += ror<4>(c0[e]); c1[e] += ror<4>(c1[e]);
    }
    u32x4 o = canon4(sel(u & 1, c1, c0));
    PIN(o);
    if (u < 2) stc4(ws, o5s, o);
  } else if (ffn) {
    const u32 of = W_HF + h * 8192 + lane * 16;
    const u32 f2 = (((g & 1) << 1) | (tq >> 1)) << 1;
    u32x4 p[8], fa[8];
    u32x2 b1v;
    ldg2(b1v, a.b1, (h * 64 + u * 16 + g * 4) * 2);
    for (;;) {
      ldc4<0>(p[0], ws, of); ldc4<1024>(p[1], ws, of); ldc4<2048>(p[2], ws, of); ldc4<3072>(p[3], ws, of);
      ldc4<0>(p[4], ws, of + 4096); ldc4<1024>(p[5], ws, of + 4096); ldc4<2048>(p[6], ws, of + 4096); ldc4<3072>(p[7], ws, of + 4096);
#pragma unroll
      for (int t = 0; t < 4; ++t) {
        const u32 ta = L_W2 + (g * 4 + tq) * 128 + (((2 * t + (tp >> 1)) ^ f2) << 4) + (tp & 1) * 8;
        fa[2 * t] = ltr(ta, ta + 2048);
        fa[2 * t + 1] = ltr(ta + 4096, ta + 4096 + 2048);
      }
      WAITV(0);
      u32 t = 0;
#pragma unroll
      for (int i = 0; i < 8; ++i) { PIN(p[i]); t = umax(t, umax4(p[i])); }
      if (none(t == SENT) || ++spins > SPIN_MAX) break;
    }
    PIN(b1v);
    const float bb[4] = {lo(b1v.x), hi(b1v.x), lo(b1v.y), hi(b1v.y)};
    float ge[4];
#pragma unroll
    for (int e = 0; e < 4; ++e) {
      const float s = ((uf(p[0][e]) + uf(p[1][e])) + (uf(p[2][e]) + uf(p[3][e]))) + ((uf(p[4][e]) + uf(p[5][e])) + (uf(p[6][e]) + uf(p[7][e])));
      const float t = rbf(rbf(s) + bb[e]);
      const float z = ex2(2.302208198f * (t + 0.044715f * t * t * t));
      ge[e] = t - t * rcp(z + 1.f);
    }
    lw2(L_HB + (u >> 1) * 256 + bfr + (u & 1) * 8, u32x2{pk(ge[0], ge[1]), pk(ge[2], ge[3])});
    asm volatile("s_waitcnt lgkmcnt(0)" ::: "memory");
    const u32x4 hb0 = lr4(L_HB + bfr), hb1 = lr4(L_HB + 256 + bfr);
    f32x4 d[4];
#pragma unroll
    for (int t = 0; t < 4; ++t) d[t] = mfma(fa[2 * t], hb0, f32x4{0, 0, 0, 0});
#pragma unroll
    for (int t = 0; t < 4; ++t) d[t] = mfma(fa[2 * t + 1], hb1, d[t]);
    stc4(ws, W_HY + o5, canon4(sel(u & 2, sel(u & 1, d[3], d[2]), sel(u & 1, d[1], d[0]))));
    STAMP(15);
  } else if (fin) {
    u32 fy = W_HY + (j * 8 + (h & 7)) * 4096 + lane * 16, fy3 = lane < 32 ? fy + 3072 : fy, fs = fy + (W_H5 - W_HY);
    u32 oo = (fb * 512 + fm) * 2;
    u32 xo, b2o;
    ldgh(xo, a.x, oo);
    ldgh(b2o, a.b2, fm * 2);
    PIN(fy); PIN(fy3); PIN(fs); PIN(oo);
    nap<P_D5>();
    u32x4 p0, p1, p2, p3;
    for (;;) {
      ldc4<0>(p0, ws, fy); ldc4<1024>(p1, ws, fy); ldc4<2048>(p2, ws, fy); ldc4<0>(p3, ws, fy3);
      WAITV(0);
      PIN(p0); PIN(p1); PIN(p2); PIN(p3);
      if (none(umax(umax(umax4(p0), umax4(p1)), umax(umax4(p2), umax4(p3))) == SENT) || ++spins > SPIN_MAX) break;
    }
    STAMP(15);
    u32x4 q0, q1, q2, q3;
    ldc4<0>(q0, ws, fs); ldc4<1024>(q1, ws, fs); ldc4<2048>(q2, ws, fs); ldc4<3072>(q3, ws, fs);
    PIN(xo); PIN(b2o);
    const f32x4 z = {0, 0, 0, 0};
    const f32x4 sy = __builtin_bit_cast(f32x4, p0) + __builtin_bit_cast(f32x4, p1) + __builtin_bit_cast(f32x4, p2) + sel(lane < 32, __builtin_bit_cast(f32x4, p3), z);
    const float y = rbf(rbf(red4(sy)) + lo(b2o));
    float gate = rbf(y * rcp(1.f + ex2(-1.4426950408889634f * y)));
    PIN(gate);
    WAITV(0);
    for (;;) {
      PIN(q0); PIN(q1); PIN(q2); PIN(q3);
      if (none(umax(umax(umax4(q0), umax4(q1)), umax(umax4(q2), umax4(q3))) == SENT) || ++spins > SPIN_MAX) break;
      ldc4<0>(q0, ws, fs); ldc4<1024>(q1, ws, fs); ldc4<2048>(q2, ws, fs); ldc4<3072>(q3, ws, fs);
      WAITV(0);
    }
    const f32x4 s = (__builtin_bit_cast(f32x4, q0) + __builtin_bit_cast(f32x4, q1)) + (__builtin_bit_cast(f32x4, q2) + __builtin_bit_cast(f32x4, q3));
    const float o = lo(xo) + rbf(rbf(red4(s)) * gate);
    u32 ov = pk(o, o);
    PIN(ov);
    if (!(lane & 8)) asm volatile("global_store_short %0, %1, %2 " OUTPOL ::"v"(oo), "v"(ov), "s"(a.out) : "memory");
    if (h == 24 && j == 0 && lane == 0) stc1(ws, 0, tag);
  }
  if (fin && EXP == 0) {
    u32x2 v; ldc2<0>(v, ws, 64u << 20); WAITV(0); PIN(v);
    const u32 t = (u32)__builtin_amdgcn_s_memrealtime(), d = (at0 - __builtin_amdgcn_readfirstlane(v.x)) * 8;
    while ((u32)__builtin_amdgcn_s_memrealtime() - t < (d < 40000 ? d : 40000)) {}
  }
  if (fin && EXP == 2 && h == 24 && j == 0) stc1(ws, 64u << 20, (u32)__builtin_amdgcn_s_memrealtime());
}

__global__ void pre(char* ws) { stc1(ws, 64u << 20, (u32)__builtin_amdgcn_s_memrealtime()); WAITV(0); }
__global__ void post(char* ws) {
  const u32 t = (u32)__builtin_amdgcn_s_memrealtime();
  u32x2 v; ldc2<0>(v, ws, 64u << 20); WAITV(0); PIN(v);
  const u32 d = (t - __builtin_amdgcn_readfirstlane(v.x)) * 8;
  while ((u32)__builtin_amdgcn_s_memrealtime() - t < (d < 40000 ? d : 40000)) {}
}
extern "C" void kernel_launch(void* const* d_in, const int* in_sizes, int n_in, void* d_out, int out_size, void* d_ws, size_t ws_size, hipStream_t stream) {
  Args a;
  a.x = (const char*)d_in[0];
  a.ckv = (const char*)d_in[1];
  a.wq = (const char*)d_in[2];
  a.wuk = (const char*)d_in[3];
  a.wuv = (const char*)d_in[4];
  a.wo = (const char*)d_in[5];
  a.w1 = (const char*)d_in[7];
  a.b1 = (const char*)d_in[8];
  a.w2 = (const char*)d_in[9];
  a.b2 = (const char*)d_in[10];
  a.out = (char*)d_out;
  a.ws = (char*)d_ws;
  (void)hipFuncSetAttribute((const void*)mla_ffn_decode, hipFuncAttributeMaxDynamicSharedMemorySize, L_TOTAL);
  if (EXP <= 1) pre<<<1, 64, 0, stream>>>(a.ws);
  mla_ffn_decode<<<256, 512, L_TOTAL, stream>>>(a);
  if (EXP == 2) post<<<1, 64, 0, stream>>>(a.ws);
}
```

```cpp
#include <hip/hip_runtime.h>
#include <hip/hip_bf16.h>
#include <stdint.h>


typedef unsigned u32;
typedef u32 u32x2 __attribute__((ext_vector_type(2)));
typedef u32 u32x4 __attribute__((ext_vector_type(4)));
typedef float f32x2 __attribute__((ext_vector_type(2)));
typedef float f32x4 __attribute__((ext_vector_type(4)));
typedef __bf16 bf16x2 __attribute__((ext_vector_type(2)));
typedef __bf16 bf16x8 __attribute__((ext_vector_type(8)));
typedef short s16x4 __attribute__((ext_vector_type(4)));
#define LAS __attribute__((address_space(3)))
#define DI __device__ __forceinline__

constexpr u32 SPIN_MAX = 1u << 16;
constexpr u32 SENT = 0xFFFFFFFFu;
constexpr int NAP_H1 = 23, NAP_H2 = 4, NAP_H4 = 18, NAP_H5 = 34;
constexpr int T_H2 = 252, T_H3 = 190, T_BURST = 65, T_KT = 520, T_KT_EARLY = 405;
constexpr int KV_INFLIGHT = 6;

constexpr u32 W_TM = 0x1000, W_KT = 0x2000, W_H1 = 0x3000, W_H2 = W_H1 + 0x80000, W_ST = W_H2 + 0x40000, W_HF = W_ST + 0x80000;
constexpr u32 W_H4 = W_HF + 0x40000, W_H5 = W_H4 + 0x40000, W_HY = W_H5 + 0x40000, W_H3 = W_HY + 0x40000;

constexpr u32 L_X = 131072, L_Y = L_X + 16384, L_TOTAL = L_Y + 16384;
constexpr u32 L_Q0 = 31 * 1024, L_XN = 63 * 1024, L_FL = 95 * 1024;
constexpr u32 L_W1 = L_X, L_MX = L_X + 8192, L_P = L_X + 9216, L_CB = L_X + 13312, L_HB = L_X + 15360;
constexpr u32 L_WO = 0, L_W2 = 16384;

struct Args {
  const char *x, *ckv, *wq, *wuk, *wuv, *wo, *w1, *b1, *w2, *b2;
  char *out, *ws;
};

#define WAITV(n) asm volatile("s_waitcnt vmcnt(%0)" ::"i"(n) : "memory")
#define BARRIER() asm volatile("s_waitcnt lgkmcnt(0)\n\ts_barrier" ::: "memory")
#define PIN(v) asm volatile("" : "+v"(v))
#define FENCE() __builtin_amdgcn_sched_barrier(0)

template <int NT> DI void dma(const char* base, u32 voff, u32 lds) {
  u32 keep;
  lds = __builtin_amdgcn_readfirstlane(lds);
  if (NT)
    asm volatile("s_mov_b32 %0, m0\n\ts_mov_b32 m0, %3\n\ts_nop 0\n\tglobal_load_lds_dwordx4 %1, %2 nt\n\ts_mov_b32 m0, %0"
                 : "=&s"(keep) : "v"(voff), "s"(base), "s"(lds) : "memory");
  else
    asm volatile("s_mov_b32 %0, m0\n\ts_mov_b32 m0, %3\n\ts_nop 0\n\tglobal_load_lds_dwordx4 %1, %2\n\ts_mov_b32 m0, %0"
                 : "=&s"(keep) : "v"(voff), "s"(base), "s"(lds) : "memory");
}
template <int OFF> DI void ldc4(u32x4& d, const char* base, u32 voff) {
  asm volatile("global_load_dwordx4 %0, %1, %2 offset:%3 sc1" : "=&v"(d) : "v"(voff), "s"(base), "i"(OFF) : "memory");
}
template <int OFF> DI void ldc2(u32x2& d, const char* base, u32 voff) {
  asm volatile("global_load_dwordx2 %0, %1, %2 offset:%3 sc1" : "=&v"(d) : "v"(voff), "s"(base), "i"(OFF) : "memory");
}
DI void ldg4(u32x4& d, const char* base, u32 voff) {
  asm volatile("global_load_dwordx4 %0, %1, %2" : "=&v"(d) : "v"(voff), "s"(base) : "memory");
}
DI void ldg2(u32x2& d, const char* base, u32 voff) {
  asm volatile("global_load_dwordx2 %0, %1, %2" : "=&v"(d) : "v"(voff), "s"(base) : "memory");
}
DI void ldgh(u32& d, const char* base, u32 voff) {
  asm volatile("global_load_ushort %0, %1, %2" : "=&v"(d) : "v"(voff), "s"(base) : "memory");
}
DI void stc4(const char* base, u32 voff, u32x4 v) {
  asm volatile("global_store_dwordx4 %0, %1, %2 sc1\n\ts_nop 1" ::"v"(voff), "v"(v), "s"(base) : "memory");
}
DI void stc2(const char* base, u32 voff, u32x2 v) {
  asm volatile("global_store_dwordx2 %0, %1, %2 sc1" ::"v"(voff), "v"(v), "s"(base) : "memory");
}
DI void stc1(const char* base, u32 voff, u32 v) {
  asm volatile("global_store_dword %0, %1, %2 sc1" ::"v"(voff), "v"(v), "s"(base) : "memory");
}

DI u32x4 lr4(u32 a) { return *(LAS u32x4*)a; }
DI void lw4(u32 a, u32x4 v) { *(LAS u32x4*)a = v; }
DI void lw2(u32 a, u32x2 v) { *(LAS u32x2*)a = v; }
DI void lw1(u32 a, u32 v) { *(LAS u32*)a = v; }
DI u32x4 ltr(u32 a0, u32 a1) {
  u32x2 p = __builtin_bit_cast(u32x2, __builtin_amdgcn_ds_read_tr16_b64_v4i16((LAS s16x4*)a0));
  u32x2 q = __builtin_bit_cast(u32x2, __builtin_amdgcn_ds_read_tr16_b64_v4i16((LAS s16x4*)a1));
  return u32x4{p.x, p.y, q.x, q.y};
}
DI f32x4 mfma(u32x4 a, u32x4 b, f32x4 c) {
  return __builtin_amdgcn_mfma_f32_16x16x32_bf16(__builtin_bit_cast(bf16x8, a), __builtin_bit_cast(bf16x8, b), c, 0, 0, 0);
}
DI u32 fu(float f) { return __float_as_uint(f); }
DI float uf(u32 u) { return __uint_as_float(u); }
DI u32 pk(float a, float b) { return __builtin_bit_cast(u32, __builtin_convertvector(f32x2{a, b}, bf16x2)); }
DI float lo(u32 u) { return uf(u << 16); }
DI float hi(u32 u) { return uf(u & 0xffff0000u); }
DI float rbf(float a) { return lo(pk(a, a)); }
DI u32x4 bits4(f32x4 v) { return __builtin_bit_cast(u32x4, v); }
DI float max3(float a, float b, float c) { float r; asm("v_max3_f32 %0, %1, %2, %3" : "=v"(r) : "v"(a), "v"(b), "v"(c)); return r; }
#pragma float_control(push)
#pragma float_control(precise, off)
DI float fmx(float a, float b) { return __builtin_fmaxf(a, b); }
#pragma float_control(pop)
DI u32 umax(u32 a, u32 b) { return a > b ? a : b; }
template <int C> DI u32 dpp(u32 v) { return (u32)__builtin_amdgcn_update_dpp(0, (int)v, C, 0xf, 0xf, true); }
DI u32 rotr(u32 v, u32 n) { return __builtin_amdgcn_alignbit(v, v, n); }
DI u32 hash4(u32x4 v, u32 rc) { return rotr(v.x + rotr(v.y, 7) + rotr(v.z, 14) + rotr(v.w, 21), rc); }
DI int imax(int a, int b) { return a > b ? a : b; }
DI u32 umin(u32 a, u32 b) { return a < b ? a : b; }
DI u32 umax4(u32x4 v) { return umax(umax(v.x, v.y), umax(v.z, v.w)); }
template <int C> DI float ror(float v) { return uf((u32)__builtin_amdgcn_update_dpp(0, (int)fu(v), 0x120 + C, 0xf, 0xf, true)); }
template <int BANKS> DI u32 ror8m(u32 keep, u32 src) { return (u32)__builtin_amdgcn_update_dpp((int)keep, (int)src, 0x128, 0xf, BANKS, false); }
DI float add32(float a, float b) { auto s = __builtin_amdgcn_permlane32_swap(fu(a), fu(b), false, false); return uf(s[0]) + uf(s[1]); }
DI float add16(float a, float b) { auto s = __builtin_amdgcn_permlane16_swap(fu(a), fu(b), false, false); return uf(s[0]) + uf(s[1]); }
DI float max16(float a) { auto s = __builtin_amdgcn_permlane16_swap(fu(a), fu(a), false, false); return fmx(uf(s[0]), uf(s[1])); }
DI float wsum(float v) {
  v += ror<8>(v); v += ror<4>(v); v += ror<2>(v); v += ror<1>(v);
  v = add16(v, v);
  return add32(v, v);
}
DI f32x4 sel(bool c, f32x4 a, f32x4 b) { return f32x4{c ? a.x : b.x, c ? a.y : b.y, c ? a.z : b.z, c ? a.w : b.w}; }
DI void lds_signal(u32 a) {
  asm volatile("s_waitcnt lgkmcnt(0)" ::: "memory");
  lw1(a, 1u);
}
DI void lds_wait4(u32 a, u32& spins) {
  for (;;) {
    asm volatile("" ::: "memory");
    const u32x4 f = lr4(a);
    if (__builtin_amdgcn_readfirstlane(f.x & f.y & f.z & f.w) || ++spins > SPIN_MAX) break;
  }
  asm volatile("" ::: "memory");
}
DI bool none(bool bad) { return __builtin_amdgcn_ballot_w64(bad) == 0; }
DI float ex2(float v) { return __builtin_amdgcn_exp2f(v); }
DI float rcp(float v) { return __builtin_amdgcn_rcpf(v); }
DI u32 now() { return (u32)__builtin_amdgcn_s_memrealtime(); }
DI void until(u32 t) {
  for (;;) {
    asm volatile("" ::: "memory");
    if ((int)(now() - t) >= 0) break;
  }
}
template <int N> DI void nap() { __builtin_amdgcn_s_sleep(N); }

DI void red8(const float (&a)[8], float& o0, float& o1) {
  float r0 = add32(a[0], a[4]), r1 = add32(a[1], a[5]), r2 = add32(a[2], a[6]), r3 = add32(a[3], a[7]);
  float s0 = add16(r0, r2), s1 = add16(r1, r3);
  o0 = s0 + ror<8>(s0);
  o1 = s1 + ror<8>(s1);
}
DI float red4(f32x4 a) {
  float r0 = add32(a.x, a.z), r1 = add32(a.y, a.w);
  float s = add16(r0, r1);
  return s + ror<8>(s);
}

extern "C" __global__ __launch_bounds__(512, 2) void mla_ffn_decode(Args a) {
  const u32 tid = threadIdx.x, bid = blockIdx.x, lane = tid & 63;
  const u32 w = __builtin_amdgcn_readfirstlane(tid >> 6);
  const u32 t0 = now();
  const u32 xcd = bid & 7, r = bid >> 3, grp = r & 1, j = (r >> 1) & 7, h = grp * 16 + xcd * 2 + (r >> 4);
  const u32 ab = xcd >> 1, aks = (xcd & 1) * 16 + (r >> 1), hg = 1 - grp, sp = ab * 32 + aks;
  const u32 mq = j >> 1, dh = j & 1;
  const u32 l15 = lane & 15, g = lane >> 4, bq = lane & 3, u = (lane >> 2) & 3, l7 = lane & 7, l3 = lane >> 3;
  const u32 tq = u, tp = bq;
  const bool ffn = w == 3 && h < 28, fin = w == 7 && h >= 24;
  const char* ws = a.ws;
  u32 tag, epoch;
#define EPOCH_LD() asm volatile("s_load_dword %0, %1, 0x0" : "=s"(epoch) : "s"(ws) : "memory")
#define TOP_SYNC() asm volatile("s_waitcnt lgkmcnt(0)\n\ts_add_u32 %0, %1, 1\n\ts_barrier" : "=s"(tag) : "s"(epoch) : "memory", "scc")
  u32 spins = 0;

  const u32 f1 = (l3 >> 1) & 1;
  const u32 trn = (g * 8 + tq) * 128 + (tp & 1) * 8, trf = ((tq >> 1) | ((g & 1) << 1)) << 1;
  const u32x4 sent4 = {SENT, SENT, SENT, SENT};
  u32 o5, ohf, ost, o3, o3b, bfr, va, frag[4];
  u32x4 xf0, xf1, lastv;
  u32 tref;
#define COMMON_ADDR()                                                                                        \
  do {                                                                                                       \
    o5 = (j * 8 + bq * 2 + (u >> 1)) * 4096 + h * 128 + (u & 1) * 64 + g * 16;                               \
    ohf = W_HF + h * 8192 + j * 1024 + lane * 16;                                                            \
    ost = W_ST + ((hg * 16 + l15) * 8 + w) * 2048 + sp * 16;                                                 \
    o3 = W_H3 + (((hg * 16 + l7) * 8 + w) * 128 + sp) * 128 + (((lane >> 3) & 1) * 4 + g) * 16;              \
    o3b = o3 + 8 * 8 * 16384;                                                                                \
    bfr = g * 64 + bq * 16;                                                                                  \
    va = (g * 4 + tq) * 1024 + (((w * 8 + (tp >> 1)) ^ (((g & 1) * 4 + tq) << 1)) << 4) + (tp & 1) * 8;      \
    _Pragma("unroll") for (int i = 0; i < 4; ++i) frag[i] = (l15 * 1024 + ((g ^ (l7 << 1)) << 4)) ^ (i << 6); \
    PIN(o5); PIN(ohf); PIN(ost); PIN(o3); PIN(o3b); PIN(bfr); PIN(va);                                       \
    PIN(frag[0]); PIN(frag[1]); PIN(frag[2]); PIN(frag[3]);                                                  \
  } while (0)
  const char* kvb = a.ckv + (size_t)(ab * 4096 + aks * 128 + (w & 3) * 32) * 1024;

  if (__builtin_expect(w < 4, 1)) {
    u32x4 xv;
    ldg4(xv, a.x, w * 1024 + lane * 16);
    const u32 vq = ((mq * 128 + w * 32 + l3) * 32 + h) * 256 + dh * 128;
    const u32 vq0 = vq + ((l7 ^ (f1 << 1)) << 4), vq1 = vq + 8 * 8192 + ((l7 ^ ((f1 | 2) << 1)) << 4);
    dma<1>(a.wq, vq0, L_X + w * 4096);
    dma<1>(a.wq, vq1, L_X + w * 4096 + 1024);
    dma<1>(a.wq + 16 * 8192, vq0, L_X + w * 4096 + 2048);
    dma<1>(a.wq + 16 * 8192, vq1, L_X + w * 4096 + 3072);
#define UK_DMA()                                                                                              \
  _Pragma("unroll") for (int i = 0; i < 4; ++i)                                                               \
      dma<1>(a.wuk + i * 4 * 8192, ((j * 64 + w * 16 + g) * 32 + h) * 256 + ((l15 ^ (i * 4 + g)) << 4), L_Y + w * 4096 + i * 1024)
    EPOCH_LD();
    TOP_SYNC();
    COMMON_ADDR();
#define OC(v) ((v) == 0 ? ohf : (v) == 1 ? W_H4 + h * 8192 + l3 * 1024 + j * 128 + l7 * 16 : (v) == 2 ? W_H5 + o5 : W_HY + o5)
    u32 taq = L_X + trn + (((2 * w + (tp >> 1)) ^ trf) << 4);
    u32 o1s = W_H1 + h * 16384 + dh * 8192 + w * 2048 + mq * 512 + (g * 8 + l7) * 16;
    u32 o1 = W_H1 + h * 16384 + w * 4096 + lane * 16;
    u32 iq = L_Q0 + (4 * w + (lane >> 5) * 2 + (g & 1)) * 64 + bq * 16 + (l3 & 1) * 8 + (u & 1) * 4;
    u32 ua = L_Y + (w * 32 + l15) * 256 + ((g ^ l15) << 4);
    u32 o2s = W_H2 + (bq * 32 + h) * 2048 + j * 256 + w * 128 + u * 64 + g * 16;
    u32 o2 = W_H2 + (ab * 32 + hg * 16 + 4 * w) * 2048 + lane * 16, o2b = o2 + 4096;
    u32 i0 = L_X + 4 * w * 1024 + (((lane >> 1) ^ (((4 * w) & 7) << 1)) << 4) + (lane & 1) * 8;
    PIN(taq); PIN(o1s); PIN(o1); PIN(iq); PIN(ua); PIN(o2s); PIN(o2); PIN(o2b); PIN(i0);

    WAITV(4);
    PIN(xv);
    {
      float v[8] = {lo(xv.x), hi(xv.x), lo(xv.y), hi(xv.y), lo(xv.z), hi(xv.z), lo(xv.w), hi(xv.w)};
      const float mean = wsum(((v[0] + v[1]) + (v[2] + v[3])) + ((v[4] + v[5]) + (v[6] + v[7]))) * (1.f / 512.f);
      float d[8];
#pragma unroll
      for (int i = 0; i < 8; ++i) { d[i] = v[i] - mean; d[i] *= d[i]; }
      const float var = rbf(wsum(((d[0] + d[1]) + (d[2] + d[3])) + ((d[4] + d[5]) + (d[6] + d[7]))) * (1.f / 512.f)), mb = rbf(mean);
      const float inv = rcp(rbf(__builtin_amdgcn_sqrtf(rbf(var + 1e-5f))));
      u32x4 o;
#pragma unroll
      for (int i = 0; i < 4; ++i) { const u32 c = pk(v[2 * i] - mb, v[2 * i + 1] - mb); o[i] = pk(lo(c) * inv, hi(c) * inv); }
      PIN(o);
      if (g == mq) lw4(L_XN + l15 * 64 + w * 16, o);
    }
    WAITV(0);
    lds_signal(L_FL + w * 4);
    UK_DMA();
    lds_wait4(L_FL, spins);
    {
      u32x4 fa[4], fb[4];
#pragma unroll
      for (int ks = 0; ks < 4; ++ks) { fa[ks] = ltr(taq + ks * 4096, taq + ks * 4096 + 512); fb[ks] = lr4(L_XN + ks * 256 + bfr); }
      FENCE();
      f32x4 c0 = mfma(fa[0], fb[0], f32x4{0, 0, 0, 0}), c1 = mfma(fa[1], fb[1], f32x4{0, 0, 0, 0});
      c0 = mfma(fa[2], fb[2], c0);
      c1 = mfma(fa[3], fb[3], c1);
      xf0 = dh ? fb[2] : fb[0];
      xf1 = dh ? fb[3] : fb[1];
      const f32x4 acc = c0 + c1;
      u32x4 o = {fu((u & 1) ? acc.z : acc.x), tag, fu((u & 1) ? acc.w : acc.y), tag};
      PIN(o);
      if (u < 2) stc4(ws, o1s, o);
      if (w == 3 && j == 0) { u32x4 tm = {now(), tag, 0, tag}; PIN(tm); if (lane < 8) stc4(ws, W_TM + h * 128 + lane * 16, tm); }
    }
    lw1(L_FL + 32 + w * 4, 1u);
    u32x2 tm;
    u32 otm = W_TM + (hg * 16 + l15) * 128;
    PIN(otm);
    {
      u32x4 p0, p1, p2, p3;
      nap<NAP_H1>();
      ldc2<0>(tm, ws, otm);
      ldc4<0>(p0, ws, o1); ldc4<1024>(p1, ws, o1); ldc4<2048>(p2, ws, o1); ldc4<3072>(p3, ws, o1);
      WAITV(0);
      PIN(tm);
      for (;;) {
        PIN(p0); PIN(p1); PIN(p2); PIN(p3);
        const u32 bad = (p0.y ^ tag) | (p0.w ^ tag) | (p1.y ^ tag) | (p1.w ^ tag) | (p2.y ^ tag) | (p2.w ^ tag) | (p3.y ^ tag) | (p3.w ^ tag);
        if (none(bad != 0) || ++spins > SPIN_MAX) break;
        ldc4<0>(p0, ws, o1); ldc4<1024>(p1, ws, o1); ldc4<2048>(p2, ws, o1); ldc4<3072>(p3, ws, o1);
        WAITV(0);
      }
      lw1(iq, pk(add32(uf(p0.x) + uf(p1.x), uf(p2.x) + uf(p3.x)), add32(uf(p0.z) + uf(p1.z), uf(p2.z) + uf(p3.z))));
    }
    lds_signal(L_FL + 16 + w * 4);
    if (w < 2) {
      u32x4 fb[4], f0[4], f1v[4];
      lds_wait4(L_FL + 16, spins);
#pragma unroll
      for (int ks = 0; ks < 4; ++ks) { fb[ks] = lr4(L_Q0 + bfr + ks * 256); f0[ks] = lr4(ua ^ (ks << 6)); f1v[ks] = lr4((ua ^ (ks << 6)) + 4096); }
      FENCE();
      f32x4 c0 = {0, 0, 0, 0}, c1 = {0, 0, 0, 0};
#pragma unroll
      for (int ks = 0; ks < 4; ++ks) { c0 = mfma(f0[ks], fb[ks], c0); c1 = mfma(f1v[ks], fb[ks], c1); }
      const float sc = 0.08838834764831845f * 1.4426950408889634f;
      const f32x4 c = sel(u & 1, c1, c0) * sc;
      u32x4 o = {pk(c.x, c.y), tag, pk(c.z, c.w), tag};
      PIN(o);
      lds_wait4(L_FL + 48, spins);
      if (u < 2) stc4(ws, o2s, o);
      nap<NAP_H2>();
    } else {
      lds_wait4(L_FL + 16, spins);
      nap<NAP_H2 + 6>();
    }
#define LD8(f, s, a, b, o1, o2, o3) do { f<0>(s[0], ws, a); f<o1>(s[1], ws, a); f<o2>(s[2], ws, a); f<o3>(s[3], ws, a); \
                                         f<0>(s[4], ws, b); f<o1>(s[5], ws, b); f<o2>(s[6], ws, b); f<o3>(s[7], ws, b); } while (0)
    u32x4 s[8];
    if (!none(tm.y != tag)) {
      ldc2<0>(tm, ws, otm);
      WAITV(0);
      PIN(tm);
    }
    {
      int dt = (int)((tm.y == tag ? tm.x : now()) - t0);
      dt = imax(dt, (int)__builtin_amdgcn_update_dpp(0, dt, 0x128, 0xf, 0xf, true));
      dt = imax(dt, (int)__builtin_amdgcn_update_dpp(0, dt, 0x124, 0xf, 0xf, true));
      dt = imax(dt, (int)__builtin_amdgcn_update_dpp(0, dt, 0x122, 0xf, 0xf, true));
      dt = imax(dt, (int)__builtin_amdgcn_update_dpp(0, dt, 0x121, 0xf, 0xf, true));
      tref = t0 + __builtin_amdgcn_readfirstlane(dt < 1000 ? dt : 1000);
      if (w == 0) lw1(L_FL + 64, tref);
    }
    until(tref + T_H2);
    LD8(ldc4, s, o2, o2b, 1024, 2048, 3072);
    WAITV(0);
    for (;;) {
      u32 bad = 0;
#pragma unroll
      for (int i = 0; i < 8; ++i) { PIN(s[i]); bad |= (s[i].y ^ tag) | (s[i].w ^ tag); }
      if (none(bad != 0) || ++spins > SPIN_MAX) break;
      LD8(ldc4, s, o2, o2b, 1024, 2048, 3072);
      WAITV(0);
    }
#pragma unroll
    for (int i = 0; i < 8; ++i) lw2((i0 ^ ((i >> 1) << 5)) + (i >> 1) * 1024 + (i & 1) * 512, u32x2{s[i].x, s[i].z});
    BARRIER();
    tref = __builtin_amdgcn_readfirstlane(*(LAS u32*)(L_FL + 64));
  } else {
    if (w == 4) lw2(L_FL + lane * 8, u32x2{0, 0});
    EPOCH_LD();
    TOP_SYNC();
    COMMON_ADDR();
    stc4(ws, OC(w & 3), sent4);
    if (w == 5) {
      u32 pf0, pf1, pf2;
      const char* pc;
      u32 len;
      asm volatile("s_getpc_b64 %0\n.Lpf_here:\n\ts_mov_b32 %1, .Lpf_here-mla_ffn_decode" : "=s"(pc), "=s"(len));
      pc -= len;
      const u32 o = lane * 128;
      asm volatile("global_load_dword %0, %1, %2" : "=&v"(pf0) : "v"(umin(o, len)), "s"(pc) : "memory");
      asm volatile("global_load_dword %0, %1, %2" : "=&v"(pf1) : "v"(umin(o + 8192, len)), "s"(pc) : "memory");
      asm volatile("global_load_dword %0, %1, %2" : "=&v"(pf2) : "v"(umin(o + 16384, len)), "s"(pc) : "memory");
      WAITV(0);
      PIN(pf0); PIN(pf1); PIN(pf2);
    }
    WAITV(0);
    lw1(L_FL + 48 + (w & 3) * 4, 1u);
    lds_wait4(L_FL + 32, spins);
    const u32 lb = (w & 3) * 32768;
    u32x4 last;
    ldg4(last, kvb + 31 * 1024, (lane ^ 14) << 4);
#pragma unroll
    for (u32 row = 0; row < 31; ++row) {
      if (row >= KV_INFLIGHT) WAITV(KV_INFLIGHT - 1);
      dma<0>(kvb + row * 1024, (lane ^ ((row & 7) << 1)) << 4, lb + row * 1024);
    }
    PIN(last);
    BARRIER();
    tref = __builtin_amdgcn_readfirstlane(*(LAS u32*)(L_FL + 64));
    lastv = last;
  }
  const u32 lkv = w * 16384;
  const u32 vuk = ((j * 64 + w * 8 + g) * 32 + h) * 256;
  u32 vuv0 = vuk + ((l15 ^ (g << 1)) << 4), vuv1 = vuk + 4 * 8192 + ((l15 ^ ((4 + g) << 1)) << 4);
#define UV_DMA() do { dma<1>(a.wuv, vuv0, L_Y + w * 2048); dma<1>(a.wuv, vuv1, L_Y + w * 2048 + 1024); } while (0)
#define W1_DMA()                                                                                              \
  _Pragma("unroll") for (int i = 0; i < 8; ++i)                                                               \
      dma<1>(a.w1 + i * 8 * 3584, (j * 64 + l3) * 3584 + h * 128 + ((l7 ^ ((f1 | ((i & 1) << 1)) << 1)) << 4), L_W1 + i * 1024)
  UV_DMA();
  PIN(vuv0); PIN(vuv1);

  u32x4 fr[16];
#pragma unroll
  for (int ks = 0; ks < 16; ++ks) fr[ks] = lr4(L_X + frag[ks & 3] + (ks >> 2) * 256);
#pragma unroll
  for (int i = 0; i < 4; ++i) { frag[i] += lkv; PIN(frag[i]); }
  BARRIER();
  if (w >= 4) lw4((w & 3) * 32768 + 31 * 1024 + lane * 16, lastv);
  if (ffn) { W1_DMA(); }
  u32 imx = L_MX + l15 * 4, ip = L_P + lane * 16, ipw = ip + (w >> 1) * 1024 + (w & 1) * 8, vt[8];
#pragma unroll
  for (int i = 0; i < 8; ++i) { vt[i] = (va ^ ((i & 3) << 5)) + (i >> 2) * 65536; PIN(vt[i]); }
  PIN(imx); PIN(ip); PIN(ipw);
  if (w == 0) lw1(imx, 0xFF800000u);
  if (w >= 4) WAITV(2);
  BARRIER();
  u32 okt = W_KT + grp * 1024 + lane * 16, okt0 = okt;
  if (w == 7) { u32x2 kt = {now(), tag}; PIN(kt); if (lane == 0) stc2(ws, W_KT + hg * 1024 + sp * 8, kt); }
  if (w == 0 && (int)(now() - tref) < T_KT_EARLY) okt0 = 0x800u;
  PIN(okt0);
  f32x4 sv;
  {
    u32x4 fa[16];
#pragma unroll
    for (int ks = 0; ks < 4; ++ks) fa[ks] = lr4(frag[ks & 3] + (ks >> 2) * 256);
    f32x4 c[2] = {{0, 0, 0, 0}, {0, 0, 0, 0}};
#pragma unroll
    for (int ks = 0; ks < 16; ++ks) {
      FENCE();
      if (ks + 4 < 16) fa[ks + 4] = lr4(frag[(ks + 4) & 3] + ((ks + 4) >> 2) * 256);
      FENCE();
      c[ks & 1] = mfma(fa[ks], fr[ks], c[ks & 1]);
    }
    FENCE();
    sv = c[0] + c[1];
    asm volatile("ds_max_f32 %0, %1" ::"v"(imx), "v"(max3(max3(sv.x, sv.y, sv.z), sv.w, sv.w)) : "memory");
  }
  BARRIER();
#define PVRD(i) fr[i] = ltr(vt[((i) & 3) + ((i) >> 3) * 4] + (((i) >> 2) & 1) * 32768, vt[((i) & 3) + ((i) >> 3) * 4] + (((i) >> 2) & 1) * 32768 + 16384)
  {
    const float mx = uf(*(LAS u32*)imx);
    FENCE();
#pragma unroll
    for (int i = 0; i < 7; ++i) PVRD(i);
    FENCE();
    const f32x4 e = sv - mx;
    lw2(ipw, u32x2{pk(ex2(e.x), ex2(e.y)), pk(ex2(e.z), ex2(e.w))});
    BARRIER();
    u32x4 pf[4], kt;
#pragma unroll
    for (int ks = 0; ks < 4; ++ks) pf[ks] = lr4(ip + ks * 1024);
    f32x4 o[4] = {{0, 0, 0, 0}, {0, 0, 0, 0}, {0, 0, 0, 0}, {0, 0, 0, 0}};
#pragma unroll
    for (int i = 0; i < 16; ++i) {
      FENCE();
      if (i + 7 < 16) PVRD(i + 7);
      FENCE();
      o[i & 3] = mfma(fr[i], pf[i >> 2], o[i & 3]);
    }
    FENCE();
    const u32x4 r0 = {pk(o[0].x, o[0].y), pk(o[0].z, o[0].w), pk(o[1].x, o[1].y), pk(o[1].z, o[1].w)};
    const u32x4 r1 = {pk(o[2].x, o[2].y), pk(o[2].z, o[2].w), pk(o[3].x, o[3].y), pk(o[3].z, o[3].w)};
    u32x4 n0, n1;
#pragma unroll
    for (int e = 0; e < 4; ++e) n0[e] = ror8m<0xC>(r0[e], r1[e]);
    if (w == 0) ldc4<0>(kt, ws, okt0);
    asm volatile("global_store_dwordx4 %0, %1, %2 sc1\n\ts_nop 1" ::"v"(o3), "v"(n0), "s"(ws) : "memory");
#pragma unroll
    for (int e = 0; e < 4; ++e) n1[e] = ror8m<0x3>(r1[e], r0[e]);
    asm volatile("global_store_dwordx4 %0, %1, %2 sc1\n\ts_nop 1" ::"v"(o3b), "v"(n1), "s"(ws) : "memory");
    const u32x4 ones = {0x3F803F80u, 0x3F803F80u, 0x3F803F80u, 0x3F803F80u};
    f32x4 l0 = mfma(ones, pf[0], f32x4{0, 0, 0, 0}), l1 = mfma(ones, pf[1], f32x4{0, 0, 0, 0});
    l0 = mfma(ones, pf[2], l0);
    l1 = mfma(ones, pf[3], l1);
    const u32 rc = ((l3 & 1) * 4 + g) * 3 + 1, c0 = hash4(n0, rc), c1 = hash4(n1, rc);
    u32 c = ((lane & 8) ? c1 : c0) + dpp<0x128>((lane & 8) ? c0 : c1);
    { auto t = __builtin_amdgcn_permlane16_swap(c, c, false, false); c = t[0] + t[1]; }
    { auto t = __builtin_amdgcn_permlane32_swap(c, c, false, false); c = t[0] + t[1]; }
    const u32 lb = fu(l0.x + l1.x);
    u32x4 st = {fu(mx), lb, c + rotr(fu(mx), 5) + rotr(lb, 11) + tag, tag};
    PIN(st);
    if (lane < 16) stc4(ws, ost, st);
    if (w == 0) {
      WAITV(3);
      for (int it = 0;; ++it) {
        PIN(kt);
        if (none(((kt.y ^ tag) | (kt.w ^ tag)) != 0) || it >= 8) break;
        until(tref + T_KT);
        if (it) nap<4>();
        ldc4<0>(kt, ws, okt);
        WAITV(0);
      }
      const int e0 = kt.y == tag ? (int)(kt.x - tref) : 0, e1 = kt.w == tag ? (int)(kt.z - tref) : 0;
      int dt = imax(e0, e1), ds = e0 + e1;
      ds += (int)dpp<0x128>(ds);
      ds += (int)dpp<0x124>(ds);
      ds += (int)dpp<0x122>(ds);
      ds += (int)dpp<0x121>(ds);
      ds = (__builtin_amdgcn_readlane(ds, 0) + __builtin_amdgcn_readlane(ds, 16)) + (__builtin_amdgcn_readlane(ds, 32) + __builtin_amdgcn_readlane(ds, 48));
      dt = imax(dt, (int)dpp<0x128>(dt));
      dt = imax(dt, (int)dpp<0x124>(dt));
      dt = imax(dt, (int)dpp<0x122>(dt));
      dt = imax(dt, (int)dpp<0x121>(dt));
      dt = imax(imax(__builtin_amdgcn_readlane(dt, 0), __builtin_amdgcn_readlane(dt, 16)), imax(__builtin_amdgcn_readlane(dt, 32), __builtin_amdgcn_readlane(dt, 48)));
      dt = imax(dt, (ds >> 7) + T_BURST);
      lw1(L_MX + 64, tref + (dt < 1000 ? dt : 1000));
    }
  }
  const u32 vo = (h * 128 + w * 16 + l3) * 1024 + j * 128;
  u32 vo0 = vo + ((l7 ^ (f1 << 1)) << 4), vo1 = vo + 8 * 1024 + ((l7 ^ ((f1 | 2) << 1)) << 4);
  u32 vw2 = (h * 64 + w * 8 + l3) * 1024 + j * 128 + ((l7 ^ (l3 & 6)) << 4);
  PIN(vo0); PIN(vo1); PIN(vw2);
#define WO_DMA()                                          \
  do {                                                    \
    dma<1>(a.wo, vo0, L_WO + w * 2048);                   \
    dma<1>(a.wo, vo1, L_WO + w * 2048 + 1024);            \
    if (h < 28) dma<1>(a.w2, vw2, L_W2 + w * 1024);       \
  } while (0)
  BARRIER();
  tref = __builtin_amdgcn_readfirstlane(*(LAS u32*)(L_MX + 64));

  const u32 fb = (h >> 1) & 3, fm = j * 64 + (h & 1) * 32 + l7 * 4 + (lane >> 5) * 2 + ((lane >> 4) & 1);
  u32 osn = W_ST + (h * 8 + j) * 2048 + (w >> 1) * 512 + (lane & 31) * 16, ibp = ((w & 1) * 16 + l3) * 4;
  u32 od = W_H3 + ((h * 8 + j) * 128 + w * 16) * 128 + lane * 16;
  u32 icb = L_CB + l7 * 128 + ((((w & 1) * 4 + (w >> 1)) ^ l7) << 4) + (lane >> 5) * 8 + ((lane >> 4) & 1) * 4;
  u32 vu = L_Y + (g * 4 + tq) * 256 + (((2 * w + (tp >> 1)) ^ (((g & 1) * 4 + tq) << 1)) << 4) + (tp & 1) * 8;
  u32 rcb = L_CB + g * 128 + ((l7 ^ g) << 4);
  u32 o4s = W_H4 + h * 8192 + w * 1024 + j * 128 + bq * 32 + g * 8;
  PIN(osn); PIN(ibp); PIN(od); PIN(icb); PIN(vu); PIN(rcb); PIN(o4s);
  {
    u32x4 sa, d0, d1;
    float w0, w1;
    const u32 rc = l7 * 3 + 1;
    until(tref + T_H3);
    for (bool first = true;; first = false) {
      ldc4<0>(sa, ws, osn);
      ldc4<0>(d0, ws, od); ldc4<1024>(d1, ws, od);
      const bool dm = ffn && first;
      if (dm) {
        WAITV(6);
        u32x4 fa[8];
#pragma unroll
        for (int t = 0; t < 4; ++t) {
          const u32 ta = L_W1 + trn + (((2 * t + (tp >> 1)) ^ trf) << 4);
          fa[2 * t] = ltr(ta, ta + 512);
          fa[2 * t + 1] = ltr(ta + 4096, ta + 4096 + 512);
        }
        FENCE();
        f32x4 d[4];
#pragma unroll
        for (int t = 0; t < 4; ++t) d[t] = mfma(fa[2 * t], xf0, f32x4{0, 0, 0, 0});
#pragma unroll
        for (int t = 0; t < 4; ++t) d[t] = mfma(fa[2 * t + 1], xf1, d[t]);
        stc4(ws, ohf, bits4(sel(u & 2, sel(u & 1, d[3], d[2]), sel(u & 1, d[1], d[0]))));
        WAITV(3);
      } else {
        WAITV(2);
      }
      PIN(sa);
      const float sm = uf(sa.x), sl = uf(sa.y);
      const u32 ex = sa.z - rotr(sa.x, 5) - rotr(sa.y, 11) - tag;
      const u32 x0 = __builtin_amdgcn_ds_bpermute(ibp, ex), x1 = __builtin_amdgcn_ds_bpermute(ibp + 32, ex);
      float m = fmx(sm, ror<8>(sm));
      m = fmx(m, ror<4>(m)); m = fmx(m, ror<2>(m)); m = fmx(m, ror<1>(m));
      m = max16(m);
      const float e = ex2(sm - m);
      float l = e * sl;
      l += ror<8>(l); l += ror<4>(l); l += ror<2>(l); l += ror<1>(l);
      const u32 wt = fu(e * rcp(add16(l, l)));
      w0 = uf(__builtin_amdgcn_ds_bpermute(ibp, wt));
      w1 = uf(__builtin_amdgcn_ds_bpermute(ibp + 32, wt));
      u32 bad = sa.w ^ tag, y0 = x0, y1 = x1;
      PIN(w0); PIN(w1); PIN(y0); PIN(y1); PIN(bad);
      if (dm) WAITV(1);
      else WAITV(0);
      PIN(d0); PIN(d1);
      u32 c0 = hash4(d0, rc), c1 = hash4(d1, rc);
      c0 += dpp<0xB1>(c0); c1 += dpp<0xB1>(c1);
      c0 += dpp<0x4E>(c0); c1 += dpp<0x4E>(c1);
      c0 += dpp<0x141>(c0); c1 += dpp<0x141>(c1);
      if (none((bad | (c0 ^ y0) | (c1 ^ y1)) != 0) || ++spins > SPIN_MAX) break;
    }
    WO_DMA();
    float acc[8];
#pragma unroll
    for (int e = 0; e < 4; ++e) {
      acc[2 * e] = w0 * lo(d0[e]) + w1 * lo(d1[e]);
      acc[2 * e + 1] = w0 * hi(d0[e]) + w1 * hi(d1[e]);
    }
    float c0, c1;
    red8(acc, c0, c1);
    u32 o = pk(c0, c1);
    PIN(o);
    if (!(lane & 8)) lw1(icb, o);
  }
  BARRIER();
  {
    const u32x4 a0 = ltr(vu, vu + 4096), b0 = lr4(rcb), a1 = ltr(vu + 8192, vu + 8192 + 4096), b1 = lr4((rcb ^ 64) + 512);
    FENCE();
    const f32x4 c = mfma(a0, b0, f32x4{0, 0, 0, 0}) + mfma(a1, b1, f32x4{0, 0, 0, 0});
    u32x2 v = {pk(c.x + ror<4>(c.x), c.y + ror<4>(c.y)), pk(c.z + ror<4>(c.z), c.w + ror<4>(c.w))};
    PIN(v);
    if (u == 0) stc2(ws, o4s, v);
  }
  if (w < 2) {
    const u32 ta0 = L_WO + trn + (((4 * w + (tp >> 1)) ^ trf) << 4), ta1 = ta0 ^ 32;
    u32 o4 = W_H4 + h * 8192 + (g >> 1) * 1024 + u * 128 + bq * 32 + (g & 1) * 16, o4b = o4 + 4096;
    u32 o5s = W_H5 + (j * 8 + bq * 2 + w) * 4096 + h * 128 + u * 64 + g * 16;
    u32x4 fa[8], p[8];
    PIN(o4); PIN(o4b); PIN(o5s);
    nap<NAP_H4>();
    LD8(ldc4, p, o4, o4b, 512, 2048, 2560);
    WAITV(9);
    BARRIER();
#pragma unroll
    for (int ks = 0; ks < 4; ++ks) { fa[ks] = ltr(ta0 + ks * 4096, ta0 + ks * 4096 + 512); fa[4 + ks] = ltr(ta1 + ks * 4096, ta1 + ks * 4096 + 512); }
#pragma unroll
    for (int i = 0; i < 8; ++i) PIN(fa[i]);
    WAITV(0);
    for (;;) {
      u32 t = 0;
#pragma unroll
      for (int i = 0; i < 8; ++i) { PIN(p[i]); t = umax(t, umax4(p[i])); }
      if (none(t == SENT) || ++spins > SPIN_MAX) break;
      LD8(ldc4, p, o4, o4b, 512, 2048, 2560);
      WAITV(0);
    }
    f32x4 c[4] = {{0, 0, 0, 0}, {0, 0, 0, 0}, {0, 0, 0, 0}, {0, 0, 0, 0}};
#pragma unroll
    for (int ks = 0; ks < 4; ++ks) {
      c[0] = mfma(fa[ks], p[2 * ks], c[0]);
      c[1] = mfma(fa[4 + ks], p[2 * ks], c[1]);
      c[2] = mfma(fa[ks], p[2 * ks + 1], c[2]);
      c[3] = mfma(fa[4 + ks], p[2 * ks + 1], c[3]);
    }
    f32x4 c0 = c[0] + c[2], c1 = c[1] + c[3];
#pragma unroll
    for (int e = 0; e < 4; ++e) {
      c0[e] += ror<8>(c0[e]); c1[e] += ror<8>(c1[e]);
      c0[e] += ror<4>(c0[e]); c1[e] += ror<4>(c1[e]);
    }
    u32x4 o = bits4(sel(u & 1, c1, c0));
    PIN(o);
    if (u < 2) stc4(ws, o5s, o);
  } else if (ffn) {
    const u32 of = W_HF + h * 8192 + lane * 16;
    const u32 f2 = (((g & 1) << 1) | (tq >> 1)) << 1;
    u32x4 p[8], fa[8];
    u32x2 b1v;
    ldg2(b1v, a.b1, (h * 64 + u * 16 + g * 4) * 2);
    const u32 ofb = of + 4096;
    LD8(ldc4, p, of, ofb, 1024, 2048, 3072);
    WAITV(10);
    BARRIER();
#pragma unroll
    for (int t = 0; t < 4; ++t) {
      const u32 ta = L_W2 + (g * 4 + tq) * 128 + (((2 * t + (tp >> 1)) ^ f2) << 4) + (tp & 1) * 8;
      fa[2 * t] = ltr(ta, ta + 2048);
      fa[2 * t + 1] = ltr(ta + 4096, ta + 4096 + 2048);
    }
#pragma unroll
    for (int i = 0; i < 8; ++i) PIN(fa[i]);
    WAITV(0);
    for (;;) {
      u32 t = 0;
#pragma unroll
      for (int i = 0; i < 8; ++i) { PIN(p[i]); t = umax(t, umax4(p[i])); }
      if (none(t == SENT) || ++spins > SPIN_MAX) break;
      LD8(ldc4, p, of, ofb, 1024, 2048, 3072);
      WAITV(0);
    }
    PIN(b1v);
    const float bb[4] = {lo(b1v.x), hi(b1v.x), lo(b1v.y), hi(b1v.y)};
    float ge[4];
#pragma unroll
    for (int e = 0; e < 4; ++e) {
      const float s = ((uf(p[0][e]) + uf(p[1][e])) + (uf(p[2][e]) + uf(p[3][e]))) + ((uf(p[4][e]) + uf(p[5][e])) + (uf(p[6][e]) + uf(p[7][e])));
      const float t = rbf(rbf(s) + bb[e]);
      const float z = ex2(2.302208198f * (t + 0.044715f * t * t * t));
      ge[e] = t - t * rcp(z + 1.f);
    }
    lw2(L_HB + (u >> 1) * 256 + bfr + (u & 1) * 8, u32x2{pk(ge[0], ge[1]), pk(ge[2], ge[3])});
    asm volatile("s_waitcnt lgkmcnt(0)" ::: "memory");
    const u32x4 hb0 = lr4(L_HB + bfr), hb1 = lr4(L_HB + 256 + bfr);
    f32x4 d[4];
#pragma unroll
    for (int t = 0; t < 4; ++t) d[t] = mfma(fa[2 * t], hb0, f32x4{0, 0, 0, 0});
#pragma unroll
    for (int t = 0; t < 4; ++t) d[t] = mfma(fa[2 * t + 1], hb1, d[t]);
    stc4(ws, W_HY + o5, bits4(sel(u & 2, sel(u & 1, d[3], d[2]), sel(u & 1, d[1], d[0]))));
  } else {
    WAITV(1);
    BARRIER();
  }
  if (fin) {
    u32 fy = W_HY + (j * 8 + (h & 7)) * 4096 + lane * 16, fy3 = lane < 32 ? fy + 3072 : fy, fs = fy + (W_H5 - W_HY);
    u32 oo = (fb * 512 + fm) * 2;
    u32 xo, b2o;
    ldgh(xo, a.x, oo);
    ldgh(b2o, a.b2, fm * 2);
    PIN(fy); PIN(fy3); PIN(fs); PIN(oo);
    nap<NAP_H5>();
    u32x4 p0, p1, p2, p3, q0, q1, q2, q3;
    for (;;) {
      ldc4<0>(p0, ws, fy); ldc4<1024>(p1, ws, fy); ldc4<2048>(p2, ws, fy); ldc4<0>(p3, ws, fy3);
      ldc4<0>(q0, ws, fs); ldc4<1024>(q1, ws, fs); ldc4<2048>(q2, ws, fs); ldc4<3072>(q3, ws, fs);
      WAITV(0);
      PIN(p0); PIN(p1); PIN(p2); PIN(p3); PIN(q0); PIN(q1); PIN(q2); PIN(q3);
      const u32 t = umax(umax(umax(umax4(p0), umax4(p1)), umax(umax4(p2), umax4(p3))), umax(umax(umax4(q0), umax4(q1)), umax(umax4(q2), umax4(q3))));
      if (none(t == SENT) || ++spins > SPIN_MAX) break;
    }
    PIN(xo); PIN(b2o);
    const f32x4 z = {0, 0, 0, 0};
    const f32x4 sy = __builtin_bit_cast(f32x4, p0) + __builtin_bit_cast(f32x4, p1) + __builtin_bit_cast(f32x4, p2) + sel(lane < 32, __builtin_bit_cast(f32x4, p3), z);
    const f32x4 s = (__builtin_bit_cast(f32x4, q0) + __builtin_bit_cast(f32x4, q1)) + (__builtin_bit_cast(f32x4, q2) + __builtin_bit_cast(f32x4, q3));
    const float y = rbf(rbf(red4(sy)) + lo(b2o));
    const float gate = rbf(y * rcp(1.f + ex2(-1.4426950408889634f * y)));
    const float o = lo(xo) + rbf(rbf(red4(s)) * gate);
    u32 ov = pk(o, o);
    PIN(ov);
    if (!(lane & 8)) asm volatile("global_store_short %0, %1, %2 sc1" ::"v"(oo), "v"(ov), "s"(a.out) : "memory");
    if (h == 24 && j == 0 && lane == 0) stc1(ws, 0, tag);
  }
}

extern "C" void kernel_launch(void* const* d_in, const int* in_sizes, int n_in, void* d_out, int out_size, void* d_ws, size_t ws_size, hipStream_t stream) {
  Args a;
  a.x = (const char*)d_in[0];
  a.ckv = (const char*)d_in[1];
  a.wq = (const char*)d_in[2];
  a.wuk = (const char*)d_in[3];
  a.wuv = (const char*)d_in[4];
  a.wo = (const char*)d_in[5];
  a.w1 = (const char*)d_in[7];
  a.b1 = (const char*)d_in[8];
  a.w2 = (const char*)d_in[9];
  a.b2 = (const char*)d_in[10];
  a.out = (char*)d_out;
  a.ws = (char*)d_ws;
  (void)hipFuncSetAttribute((const void*)mla_ffn_decode, hipFuncAttributeMaxDynamicSharedMemorySize, L_TOTAL);
  mla_ffn_decode<<<256, 512, L_TOTAL, stream>>>(a);
}
```
